```python
import torch

def _softmax(x, axis=-1):
    return torch.softmax(x, dim=axis)
def _gelu(x):
    return torch.nn.functional.gelu(x, approximate="tanh")
def _silu(x):
    return torch.nn.functional.silu(x)
def _sigmoid(x):
    return torch.sigmoid(x)
def _layernorm(x, axis=-1, eps=1e-5):
    m = x.mean(dim=axis, keepdim=True)
    v = x.var(dim=axis, keepdim=True, unbiased=False)
    return (x - m) / torch.sqrt(v + eps)
def _scatter_add(tgt, idx, src, axis=0):
    return tgt.clone().index_add_(axis, idx, src)
def _segment_sum(src, seg, n):
    out = torch.zeros((n,) + src.shape[1:], dtype=src.dtype)
    return out.index_add_(0, seg, src)
def _pad(x, pw):
    flat = tuple(v for pair in reversed(pw) for v in pair)
    return torch.nn.functional.pad(x, flat)

kernel_name = 'sequential_preLN__linear_kernel_hedgehog__dense_activation_squared_relu__prompt'

def setup_inputs():

    _w = (lambda a: (lambda t: t.to(torch.bfloat16) if t.is_floating_point() else t)(torch.as_tensor(a)))
    def _A_setup():
        B, S, D, H = 1, 2048, 448, 16
        Dh = D // H
        return {
            "x": _w((lambda *s: torch.randn(*s, dtype=torch.bfloat16) * 0.05)(B, S, D)),
            "wqkv": _w((lambda *s: torch.randn(*s, dtype=torch.bfloat16) * 0.2)(D, 3 * D)),
            "w_phi": _w((lambda *s: torch.randn(*s, dtype=torch.bfloat16) * 0.2)(H, Dh, Dh) * (Dh ** -0.5)),
            "wo": _w((lambda *s: torch.randn(*s, dtype=torch.bfloat16) * 0.05)(D, D)),
        }
    _A_in = _A_setup()

    def _B_setup():
        B, S, D, F = 1, 2048, 448, 1792
        return {
            "x": _w((lambda *s: torch.randn(*s, dtype=torch.bfloat16) * 0.05)(B, S, D)),
            "w1": _w((lambda *s: torch.randn(*s, dtype=torch.bfloat16) * 0.05)(D, F)),
            "b1": _w((lambda *s: torch.randn(*s, dtype=torch.bfloat16) * 0.2)(F)),
            "w2": _w((lambda *s: torch.randn(*s, dtype=torch.bfloat16) * 0.05)(F, D)),
            "b2": _w((lambda *s: torch.randn(*s, dtype=torch.bfloat16) * 1.6)(D)),
        }
    _B_in = _B_setup()

    return {
        "A_x": _A_in["x"],
        "A_wqkv": _A_in["wqkv"],
        "A_w_phi": _A_in["w_phi"],
        "A_wo": _A_in["wo"],
        "B_x": _B_in["x"],
        "B_w1": _B_in["w1"],
        "B_b1": _B_in["b1"],
        "B_w2": _B_in["w2"],
        "B_b2": _B_in["b2"],
    }

def reference(A_x, A_wqkv, A_w_phi, A_wo, B_x, B_w1, B_b1, B_w2, B_b2):
    def _A_ref(x, wqkv, w_phi, wo):
        B, S, D = x.shape
        H = 16; Dh = D // H
        qkv = (lambda x, *d: x.permute(*d))((x @ wqkv).reshape(B, S, 3, H, Dh), 2, 0, 3, 1, 4)
        q, k, v = qkv[0], qkv[1], qkv[2]
        pq = torch.einsum("bhsd,hdf->bhsf", q, w_phi)
        pk = torch.einsum("bhsd,hdf->bhsf", k, w_phi)
        phi_q = torch.concatenate([_softmax(pq, axis=-1), _softmax(-pq, axis=-1)], axis=-1)
        phi_k = torch.concatenate([_softmax(pk, axis=-1), _softmax(-pk, axis=-1)], axis=-1)
        kv = torch.einsum("bhsf,bhsd->bhfd", phi_k, v)
        z  = torch.einsum("bhsf->bhf", phi_k)
        num = torch.einsum("bhqf,bhfd->bhqd", phi_q, kv)
        den = torch.einsum("bhqf,bhf->bhq", phi_q, z)[..., None] + 1e-6
        o = num / den
        return (lambda x, *d: x.permute(*d))(o, 0, 2, 1, 3).reshape(B, S, D) @ wo

    def _B_ref(x, w1, b1, w2, b2):
        h = torch.einsum("bsd,df->bsf", x, w1) + b1
        zero = torch.zeros_like(h)
        r = torch.where(h > zero, h, zero)
        h = r * r
        out = torch.einsum("bsf,fd->bsd", h, w2) + b2
        return out

    x = A_x
    h = x + _A_ref(_layernorm(x, axis=-1), A_wqkv, A_w_phi, A_wo)
    h2 = h + _B_ref(_layernorm(h, axis=-1), B_w1, B_b1, B_w2, B_b2)
    return h2
```

```cpp
#include <hip/hip_runtime.h>
#include <hip/hip_bf16.h>
typedef unsigned short u16;
__device__ __forceinline__ float bf2f(u16 v){ return __uint_as_float(((unsigned)v)<<16); }
__device__ __forceinline__ u16 f2bf(float f){ unsigned u=__float_as_uint(f); u += 0x7fffu + ((u>>16)&1u); return (u16)(u>>16); }
__device__ __forceinline__ float rbf(float f){ return bf2f(f2bf(f)); }
constexpr int S=2048, D=448, H=16, DH=28, F=1792;

__global__ void ln_k(const u16* x, u16* y){
  int row=blockIdx.x; int t=threadIdx.x;
  float v[7]; float s=0;
  for(int i=0;i<7;i++){ v[i]=bf2f(x[row*D+t+64*i]); s+=v[i]; }
  for(int o=32;o>0;o>>=1) s+=__shfl_xor(s,o);
  float m=s/D; float q=0;
  for(int i=0;i<7;i++){ float d=v[i]-m; q+=d*d; }
  for(int o=32;o>0;o>>=1) q+=__shfl_xor(q,o);
  float r=rsqrtf(q/D+1e-5f);
  for(int i=0;i<7;i++) y[row*D+t+64*i]=f2bf((v[i]-m)*r);
}
__global__ void gemm_k(const u16* A,const u16* B,u16* C,int N,int K,int mode,const u16* bias,const u16* res){
  int n=blockIdx.x*64+threadIdx.x; int m=blockIdx.y;
  if(n>=N) return;
  float acc=0;
  for(int k=0;k<K;k++) acc+=bf2f(A[m*K+k])*bf2f(B[k*N+n]);
  if(mode==1) acc=rbf(acc)+bf2f(res[m*N+n]);
  if(mode==2){ acc=rbf(rbf(acc)+bf2f(bias[n])); acc=acc>0?acc:0; acc=acc*acc; }
  if(mode==3){ acc=rbf(rbf(acc)+bf2f(bias[n]))+bf2f(res[m*N+n]); }
  C[m*N+n]=f2bf(acc);
}
__global__ __launch_bounds__(64) void phi_k(const u16* qkv,const u16* wphi,u16* phi){
  int idx=blockIdx.x*64+threadIdx.x;
  int which=idx/(S*H); int r=idx%(S*H); int s=r/H, h=r%H;
  const u16* q=qkv+s*3*D+which*D+h*DH;
  float p[DH]; float mx=-1e30f,mn=1e30f;
  #pragma unroll
  for(int f=0;f<DH;f++){ float a=0; _Pragma("unroll 1") for(int d=0;d<DH;d++) a+=bf2f(q[d])*bf2f(wphi[h*DH*DH+d*DH+f]); p[f]=rbf(a); mx=fmaxf(mx,p[f]); mn=fminf(mn,p[f]); }
  float s1=0,s2=0;
  #pragma unroll
  for(int f=0;f<DH;f++){ s1+=__expf(p[f]-mx); s2+=__expf(mn-p[f]); }
  u16* o=phi+(size_t)idx*56;
  #pragma unroll
  for(int f=0;f<DH;f++){ o[f]=f2bf(__expf(p[f]-mx)/s1); o[28+f]=f2bf(__expf(mn-p[f])/s2); }
}
__global__ void kv_k(const u16* phi,const u16* qkv,float* kv){
  int idx=blockIdx.x*64+threadIdx.x; if(idx>=H*56*29) return;
  int h=idx/(56*29); int r=idx%(56*29); int f=r/29, d=r%29;
  const u16* pk=phi+(size_t)S*H*56;
  float a=0;
  for(int s=0;s<S;s++){ float v= d<28? bf2f(qkv[s*3*D+2*D+h*DH+d]) : 1.f; a+=bf2f(pk[(s*H+h)*56+f])*v; }
  kv[idx]=rbf(a);
}
__global__ void o_k(const u16* phi,const float* kv,u16* o){
  int idx=blockIdx.x*64+threadIdx.x;
  int s=idx/D; int c=idx%D; int h=c/DH, d=c%DH;
  float num=0,den=0;
  for(int f=0;f<56;f++){ float p=bf2f(phi[(s*H+h)*56+f]); num+=p*kv[(h*56+f)*29+d]; den+=p*kv[(h*56+f)*29+28]; }
  o[idx]=f2bf(rbf(num)/rbf(rbf(den)+1e-6f));
}
extern "C" void kernel_launch(void* const* d_in, const int* in_sizes, int n_in, void* d_out, int out_size, void* d_ws, size_t ws_size, hipStream_t stream){
  const u16* x=(const u16*)d_in[0]; const u16* wqkv=(const u16*)d_in[1]; const u16* wphi=(const u16*)d_in[2]; const u16* wo=(const u16*)d_in[3];
  const u16* w1=(const u16*)d_in[5]; const u16* b1=(const u16*)d_in[6]; const u16* w2=(const u16*)d_in[7]; const u16* b2=(const u16*)d_in[8];
  char* ws=(char*)d_ws;
  u16* xn=(u16*)ws; u16* qkv=(u16*)(ws+(4<<20)); u16* phi=(u16*)(ws+(16<<20)); float* kv=(float*)(ws+(32<<20)); u16* o=(u16*)(ws+(36<<20)); u16* hh=(u16*)(ws+(40<<20)); u16* h1=(u16*)(ws+(48<<20));
  ln_k<<<S,64,0,stream>>>(x,xn);
  gemm_k<<<dim3(21,S),64,0,stream>>>(xn,wqkv,qkv,3*D,D,0,nullptr,nullptr);
  phi_k<<<2*S*H/64,64,0,stream>>>(qkv,wphi,phi);
  kv_k<<<(H*56*29+63)/64,64,0,stream>>>(phi,qkv,kv);
  o_k<<<S*D/64,64,0,stream>>>(phi,kv,o);
  gemm_k<<<dim3(7,S),64,0,stream>>>(o,wo,hh,D,D,1,nullptr,x);
  ln_k<<<S,64,0,stream>>>(hh,xn);
  gemm_k<<<dim3(28,S),64,0,stream>>>(xn,w1,h1,F,D,2,b1,nullptr);
  gemm_k<<<dim3(7,S),64,0,stream>>>(h1,w2,(u16*)d_out,D,F,3,b2,hh);
}
```

```cpp
#include <hip/hip_runtime.h>
#include <hip/hip_bf16.h>

typedef unsigned short u16; typedef unsigned u32; typedef unsigned long long u64;
typedef __bf16 bf16x8 __attribute__((ext_vector_type(8)));
typedef __bf16 bf16x2 __attribute__((ext_vector_type(2)));
typedef float f32x2 __attribute__((ext_vector_type(2)));
typedef float f32x4 __attribute__((ext_vector_type(4)));
typedef float f32x16 __attribute__((ext_vector_type(16)));
typedef u32 u32x4 __attribute__((ext_vector_type(4)));
typedef u32 u32x2 __attribute__((ext_vector_type(2)));
typedef short s16x4 __attribute__((ext_vector_type(4)));
typedef __amdgpu_buffer_rsrc_t rsrc_t;
#define LAS __attribute__((address_space(3)))
#define GAS __attribute__((address_space(1)))
#define DI __device__ __forceinline__

constexpr u32 WS_EPOCH = 0;
constexpr u32 WS_F0  = 4096;
constexpr u32 WS_FA  = WS_F0 + 32768;
constexpr u32 WS_FB  = WS_FA + 32768;
constexpr u32 WS_FC  = WS_FB + 32768;
constexpr u32 WS_FD  = WS_FC + 65536;
constexpr u32 WS_ST  = WS_FD + 65536;
constexpr u32 WS_WQ  = 393216;
constexpr u32 WS_WO  = WS_WQ + 1376256;
constexpr u32 WS_KVP = WS_WO + 524288;
constexpr u32 WS_W2  = WS_KVP + 1048576;
constexpr u32 WS_O   = WS_W2 + 1835008;
constexpr u32 WS_HF  = WS_O + 2097152;
constexpr u32 WS_H1  = WS_HF + 1835008;
constexpr u32 WS_END = WS_H1 + 7340032;
static_assert(WS_ST + 131072 <= WS_WQ, "ws layout");

constexpr u32 L_X    = 0;
constexpr u32 L_PHI  = 0;
constexpr u32 L_CH   = 32768;
constexpr u32 L_W1   = 0;
constexpr u32 L_UP   = 118272;
constexpr u32 L_WQQ  = L_UP;
constexpr u32 L_WPHI = L_UP + 28672;
constexpr u32 L_AUG  = L_WPHI + 2048;
constexpr u32 L_RED  = L_UP;
constexpr u32 L_CH0  = L_UP + 32768;
constexpr u32 L_KVS  = L_CH0;
constexpr u32 L_TAB  = L_CH0 + 8192;
constexpr u32 L_TOTAL = 163840;
static_assert(L_TAB + 1024 <= L_TOTAL, "lds");

constexpr u32 OOB = 0x7ffffff0u;
constexpr u32 SPIN_CAP = 1u << 20;

DI rsrc_t mkrsrc(const void* p, u32 n) { return __builtin_amdgcn_make_buffer_rsrc((void*)p, (short)0, (int)n, 0x00020000); }
DI u32x4 ld16(rsrc_t r, u32 vo, u32 so) { return __builtin_amdgcn_raw_buffer_load_b128(r, vo, so, 16); }
DI u32x4 ld16p(rsrc_t r, u32 vo, u32 so) { return __builtin_amdgcn_raw_buffer_load_b128(r, vo, so, 0); }
DI void st16(u32x4 v, rsrc_t r, u32 vo, u32 so) { asm volatile("buffer_store_dwordx4 %0, %1, %2, %3 offen sc1\n\ts_nop 1" :: "v"(v), "v"(vo), "s"(r), "s"(so) : "memory"); }
DI void st16p(u32x4 v, rsrc_t r, u32 vo, u32 so) { asm volatile("buffer_store_dwordx4 %0, %1, %2, %3 offen\n\ts_nop 1" :: "v"(v), "v"(vo), "s"(r), "s"(so) : "memory"); }
DI void dma16(rsrc_t r, u32 vo, u32 so, u32 lds) {
  asm volatile("s_mov_b32 m0, %0\n\ts_nop 0\n\tbuffer_load_dwordx4 %1, %2, %3 offen lds" :: "s"(lds), "v"(vo), "s"(r), "s"(so) : "m0", "memory");
}
template <int N> DI void waitvm() { asm volatile("s_waitcnt vmcnt(%0)" :: "n"(N) : "memory"); }
DI void bar() { asm volatile("s_waitcnt lgkmcnt(0)\n\ts_barrier" ::: "memory"); }
#define SB() __builtin_amdgcn_sched_barrier(0)

DI u32x4 lds128(u32 a) { return *(LAS u32x4*)a; }
DI f32x4 lds128f(u32 a) { return *(LAS f32x4*)a; }
DI void sts128(u32 a, u32x4 v) { *(LAS u32x4*)a = v; }
DI void sts128f(u32 a, f32x4 v) { *(LAS f32x4*)a = v; }
DI void sts64(u32 a, u32x2 v) { *(LAS u32x2*)a = v; }
DI u32x2 ldtr(u32 a) { return __builtin_bit_cast(u32x2, __builtin_amdgcn_ds_read_tr16_b64_v4i16((LAS s16x4*)a)); }
DI u32x4 ldtr2(u32 a0, u32 a1) { u32x2 x = ldtr(a0), y = ldtr(a1); return u32x4{x.x, x.y, y.x, y.y}; }
DI f32x16 mfma32(u32x4 a, u32x4 b, f32x16 c) { return __builtin_amdgcn_mfma_f32_32x32x16_bf16(__builtin_bit_cast(bf16x8, a), __builtin_bit_cast(bf16x8, b), c, 0, 0, 0); }
DI f32x4 mfma16(u32x4 a, u32x4 b, f32x4 c) { return __builtin_amdgcn_mfma_f32_16x16x32_bf16(__builtin_bit_cast(bf16x8, a), __builtin_bit_cast(bf16x8, b), c, 0, 0, 0); }
DI u32 pk(float a, float b) { return __builtin_bit_cast(u32, __builtin_convertvector(f32x2{a, b}, bf16x2)); }
DI float bflo(u32 v) { return __uint_as_float(v << 16); }
DI float bfhi(u32 v) { return __uint_as_float(v & 0xffff0000u); }
DI float dot2(u32 a, u32 b, float c) { return __builtin_amdgcn_fdot2_f32_bf16(__builtin_bit_cast(bf16x2, a), __builtin_bit_cast(bf16x2, b), c, false); }
DI f32x2 halves(float v) { auto r = __builtin_amdgcn_permlane32_swap(__float_as_uint(v), __float_as_uint(v), false, false); return f32x2{__uint_as_float(r[0]), __uint_as_float(r[1])}; }
DI f32x2 rows16(float v) { auto r = __builtin_amdgcn_permlane16_swap(__float_as_uint(v), __float_as_uint(v), false, false); return f32x2{__uint_as_float(r[0]), __uint_as_float(r[1])}; }
template <int C> DI float dpp(float v) { return __builtin_bit_cast(float, __builtin_amdgcn_mov_dpp(__builtin_bit_cast(int, v), C, 0xf, 0xf, true)); }
DI u32 hilo(float v) { u32 h = pk(v, 0.f) & 0xffffu; float r = v - bflo(h); return h | (pk(r, 0.f) << 16); }

DI void poll(const char* ws, u32 off, u64 tag, u32& dead) {
  const GAS u64* p = (const GAS u64*)(ws + off);
  for (u32 spins = dead; spins < SPIN_CAP; ++spins) {
    u64 v = __hip_atomic_load(p, __ATOMIC_RELAXED, __HIP_MEMORY_SCOPE_AGENT);
    if (__all(v == tag)) { asm volatile("" ::: "memory"); return; }
    __builtin_amdgcn_s_sleep(1);
  }
  dead = SPIN_CAP;
  asm volatile("" ::: "memory");
}
DI void publish(char* ws, u32 off, u64 tag, int lane) {
  asm volatile("s_waitcnt vmcnt(0)" ::: "memory");
  if (lane == 0) __hip_atomic_store((GAS u64*)(ws + off), tag, __ATOMIC_RELAXED, __HIP_MEMORY_SCOPE_AGENT);
  asm volatile("" ::: "memory");
}

#define STAMP(k) do {} while (0)

DI void featmap(const f32x16& p, float a, bool hi, u32* phi) {
  float t[16];
#pragma unroll
  for (int r = 0; r < 12; r++) t[r] = p[r];
#pragma unroll
  for (int r = 12; r < 16; r++) t[r] = hi ? p[0] : p[r];
  float mx = t[0], mn = t[0];
#pragma unroll
  for (int r = 1; r < 16; r++) { mx = fmaxf(mx, t[r]); mn = fminf(mn, t[r]); }
  f32x2 s = halves(mx); mx = fmaxf(s.x, s.y);
  s = halves(mn); mn = fminf(s.x, s.y);
  const float bp = -mx * a, bm = mn * a;
  const float bph = hi ? -__builtin_inff() : bp, bmh = hi ? -__builtin_inff() : bm;
  float ep[16], em[16]; float sp = 0.f, sm = 0.f;
#pragma unroll
  for (int r = 0; r < 16; r++) {
    ep[r] = __builtin_amdgcn_exp2f(fmaf(t[r], a, r < 12 ? bp : bph));
    em[r] = __builtin_amdgcn_exp2f(fmaf(t[r], -a, r < 12 ? bm : bmh));
    sp += ep[r]; sm += em[r];
  }
  s = halves(sp); sp = __builtin_amdgcn_rcpf(s.x + s.y);
  s = halves(sm); sm = __builtin_amdgcn_rcpf(s.x + s.y);
#pragma unroll
  for (int r = 0; r < 8; r++) { phi[r] = pk(ep[2 * r] * sp, ep[2 * r + 1] * sp); phi[8 + r] = pk(em[2 * r] * sm, em[2 * r + 1] * sm); }
}

__global__ __launch_bounds__(256, 1) void fused_block(const u16* __restrict__ x, const u16* __restrict__ wqkv, const u16* __restrict__ wphi,
                                                      const u16* __restrict__ wo, const u16* __restrict__ w1, const u16* __restrict__ b1,
                                                      const u16* __restrict__ w2, const u16* __restrict__ b2, u16* __restrict__ out, char* ws) {
  extern __shared__ __attribute__((aligned(16))) char smem[];
  STAMP(0);
  const int b = blockIdx.x, tid = threadIdx.x, lane = tid & 63;
  const int w = __builtin_amdgcn_readfirstlane(tid >> 6);
  const int xcd = b & 7, r = b >> 3;
  const u32 L = (u32)(size_t)(LAS char*)smem;
  const rsrc_t rws = mkrsrc(ws, WS_END);
  const rsrc_t rx = mkrsrc(x, 2048 * 896);
  u32 dead = 0;
  const bool hi = lane >= 32;
  const u32 l16 = lane * 16;

  const int i = (xcd & 1) * 8 + (r & 7), h = (xcd >> 1) * 4 + (r >> 3);
  const int c0 = ((r & 7) + 2 * (r >> 3)) % 7;
  u64 tag;
  {
    const int a = b >> 5, q = b & 31;
    const u16* src; u32 nb, pitch, t1, dstep, so[2], kA[2], kB[2], dst[2]; bool wm = false, wom = false, two = true;
    if (q < 28 && w < 2) {
      src = wqkv; nb = 448 * 2688; pitch = 2688; t1 = 56; dstep = 84 * 1024; wm = true; two = (w == 0);
      const u32 base = 16 * q * 2688 + 112 * a, d0 = WS_WQ + ((2 * a * 28 + q) * 3) * 1024;
      so[0] = base + (w == 0 ? 896 : 0); so[1] = base + 1792;
      dst[0] = d0 + (w == 0 ? 0 : 2048); dst[1] = d0 + 1024;
      kA[0] = kA[1] = 8; kB[0] = kB[1] = 4;
    } else if (q < 28) {
      src = w2; nb = 1792 * 896; pitch = 896; t1 = 64; dstep = 1024;
#pragma unroll
      for (int u = 0; u < 2; u++) {
        const int s = 4 * q + 2 * (w - 2) + u, j = s / 7, uu = s - 7 * j;
        so[u] = (112 * j + (uu < 6 ? 32 * (uu >> 1) + 8 * (uu & 1) : 96)) * 896 + 112 * a;
        kA[u] = uu < 6 ? 4 : 8; kB[u] = uu < 6 ? 16 : 4;
        dst[u] = WS_W2 + (a * 112 + s) * 2048;
      }
    } else {
      src = wo; nb = 448 * 896; pitch = 896; t1 = 64; dstep = 1024; wom = true;
      const int hd = 4 * (q - 28) + w;
#pragma unroll
      for (int u = 0; u < 2; u++) { so[u] = (28 * hd + 16 * u) * 896 + 112 * a; kA[u] = 4; kB[u] = 8; dst[u] = WS_WO + (a * 32 + 2 * hd + u) * 2048; }
    }
    const rsrc_t rs = mkrsrc(src, nb);
    const u32 scr = L + L_WQQ + w * 4096;
#pragma unroll
    for (int u = 0; u < 2; u++) {
      if (u == 0 || two) {
        const u32 vo = (lane & 7) == 7 ? OOB : (kB[u] * (lane >> 5) + ((lane >> 3) & 3)) * pitch + (lane & 7) * 16;
        dma16(rs, vo, so[u], scr + u * 2048);
        dma16(rs, vo, so[u] + kA[u] * pitch, scr + u * 2048 + 1024);
      }
    }
    const u64 ep = __hip_atomic_load((const GAS u64*)(ws + WS_EPOCH), __ATOMIC_RELAXED, __HIP_MEMORY_SCOPE_AGENT);
    if (w == 1) {
      const rsrc_t rp = mkrsrc(wphi, 16 * 1568);
      dma16(rp, l16, h * 1568, L + L_WPHI);
      dma16(rp, l16, h * 1568 + 1024, L + L_WPHI + 1024);
    }
    const u32 xvo = (lane >> 3) * 896 + (((lane & 7) ^ (4 * (w & 1) + (lane >> 4))) * 16);
    auto xslot = [&](int s) {
      int c = s + c0; if (c >= 7) c -= 7;
      const u32 sb = (128 * i + 8 * (w & 1)) * 896 + c * 128;
#pragma unroll
      for (int g = 0; g < 8; g++) dma16(rx, xvo, sb + g * 16 * 896, L + L_X + s * 16384 + (2 * g + (w & 1)) * 1024);
    };
    if (w >= 2) { xslot(0); xslot(1); waitvm<16>(); } else waitvm<0>();
    STAMP(1);
    tag = ep * 6364136223846793005ull + 1442695040888963407ull;
    const u32 tb = scr + (8 * (lane >> 5) + ((lane >> 2) & 3)) * 128 + ((lane >> 4) & 1) * 32 + (lane & 3) * 8;
    const bool cw = wm && (lane & 31) >= 28;
#pragma unroll
    for (int u = 0; u < 2; u++) {
      if (u == 0 || two) {
        const u32 fv = (u == 1 && (lane & 31) == 28) ? 0x3f803f80u : 0u;
        const bool c1 = cw || (wom && u == 1 && hi);
        const u32 fv1 = wm ? fv : 0u;
#pragma unroll
        for (int T = 0; T < 2; T++) {
          u32x4 f = ldtr2(tb + u * 2048 + (T ? t1 : 0), tb + u * 2048 + 512 + (T ? t1 : 0));
          f.x = cw ? fv : f.x; f.y = cw ? fv : f.y; f.z = c1 ? fv1 : f.z; f.w = c1 ? fv1 : f.w;
          st16(f, rws, l16, dst[u] + T * dstep);
        }
      }
    }
    if (w >= 2) {
#pragma unroll 1
      for (int s = 2; s < 7; s++) { xslot(s); waitvm<16>(); }
    }
    publish(ws, WS_F0 + b * 128 + w * 8, tag, lane);
    STAMP(2);
  }
  bar();
  STAMP(3);

  u32x4 xf[28];
  {
    const int tok = lane & 31;
    const u32 xb = L + L_X + (4 * w + (tok >> 3)) * 1024 + (tok & 7) * 128;
    const u32 sw = (tok >> 1) & 7;
#pragma unroll
    for (int s = 0; s < 7; s++)
#pragma unroll
      for (int k = 0; k < 4; k++) xf[4 * s + k] = lds128(xb + s * 16384 + (((2 * k + (lane >> 5)) ^ sw) * 16));
  }
  u32x4 aphi[2];
  {
    const u32 pb = L + L_WPHI + (4 * (lane >> 5) + ((lane >> 2) & 3)) * 56 + ((lane >> 4) & 1) * 32 + (lane & 3) * 8;
    aphi[0] = ldtr2(pb, pb + 8 * 56);
    aphi[1] = ldtr2(pb + 16 * 56, pb + 24 * 56);
    aphi[1].z = hi ? 0u : aphi[1].z; aphi[1].w = hi ? 0u : aphi[1].w;
  }
  const u32 f0w = WS_F0 + ((h >> 1) * 32 + (lane % 28)) * 128;
  poll(ws, f0w, tag, dead);
  STAMP(4);
  u32x4 wk[7], wv[7], wq[7];
  u32 wso[7];
#pragma unroll
  for (int s = 0; s < 7; s++) { int c = s + c0; if (c >= 7) c -= 7; wso[s] = WS_WQ + ((h * 28 + 4 * c + w) * 3) * 1024; }
#pragma unroll
  for (int s = 0; s < 7; s++) { wk[s] = ld16(rws, l16, wso[s]); wv[s] = ld16(rws, l16 + 1024, wso[s]); }
  SB();
  float csk = 0.f, csv = 0.f, csq = 0.f, sxx = 0.f;
  u32 ONE2 = 0x3f803f80u, SELL = 0x00003f80u, SELH = 0x3f800000u;
  asm volatile("" : "+s"(ONE2), "+s"(SELL), "+s"(SELH));
  auto cadd = [&](float c, u32x4 f) { c = dot2(f.x, ONE2, c); c = dot2(f.y, ONE2, c); c = dot2(f.z, ONE2, c); return dot2(f.w, ONE2, c); };
  auto chaddr = [&](int s) { return L + (s == 0 ? L_CH0 : L_CH + (s - 1) * 8192) + l16; };
  auto wput = [&](int s) { sts128(chaddr(s) + w * 2048, wk[s]); sts128(chaddr(s) + w * 2048 + 1024, wv[s]); csk = cadd(csk, wk[s]); csv = cadd(csv, wv[s]); };
  f32x16 ak = {}, av = {};
  auto comp = [&](int s) {
#pragma unroll
    for (int k = 0; k < 4; k++) {
      ak = mfma32(lds128(chaddr(s) + k * 2048), xf[4 * s + k], ak);
      av = mfma32(lds128(chaddr(s) + k * 2048 + 1024), xf[4 * s + k], av);
    }
  };
#pragma unroll
  for (int k = 0; k < 28; k++) { sxx = dot2(xf[k].x, xf[k].x, sxx); sxx = dot2(xf[k].y, xf[k].y, sxx); sxx = dot2(xf[k].z, xf[k].z, sxx); sxx = dot2(xf[k].w, xf[k].w, sxx); }
  wput(0);
  bar();
  STAMP(5);
  wput(1); wput(2);
  comp(0);
  bar();
  wput(3); wput(4); wput(5); wput(6);
  {
    f32x2 s = halves(csk); *(LAS u32*)(L + L_AUG + (lane & 31) * 16 + w * 4) = hilo(s.x + s.y);
    s = halves(csv); *(LAS u32*)(L + L_AUG + 512 + (lane & 31) * 16 + w * 4) = hilo(s.x + s.y);
  }
  comp(1); comp(2);
  bar();
  poll(ws, f0w + 8, tag, dead);
#pragma unroll
  for (int s = 0; s < 7; s++) wq[s] = ld16(rws, l16 + 2048, wso[s]);
  SB();
  comp(3); comp(4); comp(5); comp(6);
  u32x4 baug; float rstd;
  {
    const float mean = halves(av[12]).y * (1.f / 448.f);
    f32x2 s = halves(sxx);
    const float var = fmaxf((s.x + s.y) * (1.f / 448.f) - mean * mean, 0.f);
    rstd = __builtin_amdgcn_rsqf(var + 1e-5f);
    const u32 hl = hilo(-mean);
    const u32 m = hi ? (hl >> 16) : (hl & 0xffffu);
    const u32 d = m | (m << 16);
    baug = u32x4{d, d, d, d};
    ak = mfma32(lds128(L + L_AUG + (lane & 31) * 16), baug, ak);
    av = mfma32(lds128(L + L_AUG + 512 + (lane & 31) * 16), baug, av);
  }
  const float asc = rstd * 1.4426950408889634f;
  STAMP(6);
  {
    f32x16 p = {};
    p = mfma32(aphi[0], u32x4{pk(ak[0], ak[1]), pk(ak[2], ak[3]), pk(ak[4], ak[5]), pk(ak[6], ak[7])}, p);
    p = mfma32(aphi[1], u32x4{pk(ak[8], ak[9]), pk(ak[10], ak[11]), pk(ak[12], ak[13]), pk(ak[14], ak[15])}, p);
    const u32 pr = L + L_PHI + (32 * w + (lane & 31)) * 200 + (lane >> 5) * 8;
    u32 vv[8];
#pragma unroll
    for (int k = 0; k < 8; k++) vv[k] = pk(av[2 * k] * rstd, av[2 * k + 1] * rstd);
    vv[6] = hi ? 0x00003f80u : vv[6];
#pragma unroll
    for (int g = 0; g < 4; g++) sts64(pr + 128 + g * 16, u32x2{vv[2 * g], vv[2 * g + 1]});
    u32 phi[16];
    featmap(p, asc, hi, phi);
#pragma unroll
    for (int g = 0; g < 8; g++) sts64(pr + g * 16, u32x2{phi[2 * g], phi[2 * g + 1]});
  }
#pragma unroll
  for (int s = 0; s < 7; s++) { sts128(L + L_WQQ + (4 * s + w) * 1024 + l16, wq[s]); csq = cadd(csq, wq[s]); }
  { f32x2 s = halves(csq); *(LAS u32*)(L + L_AUG + 1024 + (lane & 31) * 16 + w * 4) = hilo(s.x + s.y); }
  bar();
  STAMP(7);
  {
    const int g = lane >> 4, q = (lane >> 2) & 3, p = lane & 3;
    const u32 rb = L + L_PHI + ((g >> 1) + 16 * (g & 1) + 4 * q) * 200;
    const u32 ab = rb + w * 32 + (p & 1) * 16 + (p >> 1) * 8, bb = rb + 128 + p * 8;
    f32x4 k0 = {}, k1 = {};
#pragma unroll
    for (int st = 0; st < 4; st++) {
      const u32x4 A = ldtr2(ab + st * 6400, ab + st * 6400 + 400);
      k0 = mfma16(A, ldtr2(bb + st * 6400, bb + st * 6400 + 400), k0);
      k1 = mfma16(A, ldtr2(bb + st * 6400 + 32, bb + st * 6400 + 432), k1);
    }
    auto s0 = __builtin_amdgcn_permlane16_swap(pk(k0[0], k0[1]), pk(k1[0], k1[1]), false, false);
    auto s1 = __builtin_amdgcn_permlane16_swap(pk(k0[2], k0[3]), pk(k1[2], k1[3]), false, false);
    st16(u32x4{s0[0], s1[0], s0[1], s1[1]}, rws, l16, WS_KVP + ((h * 16 + i) * 4 + w) * 1024);
    publish(ws, WS_FA + (h * 16 + i) * 128 + w * 8, tag, lane);
  }
  STAMP(8);
  bar();

  const int j8 = r & 7, tq = r >> 3;
  const int t32a = 8 * xcd + tq;
  const int j16 = 8 * (xcd & 1) + (r & 7);
  const int t64a = 2 * (4 * (xcd >> 1) + 2 * (tq >> 1)) + (tq & 1);
  const int rot14 = 2 * ((r & 7) % 7);
  const int rot7 = j8 % 7;

  {
    const rsrc_t r1 = mkrsrc(w1, 448 * 3584);
    const u32 vo = ((lane >> 5) * 4 + ((lane >> 4) & 1) * 16) * 3584 + (lane & 15) * 16;
    const u32 wb = 224 * j16 - 32 * (j16 & 1) + w * 3584;
    int ks = rot14;
#pragma unroll 1
    for (int s = 0; s < 14; s++) {
      dma16(r1, vo, wb + ks * 32 * 3584, L + L_W1 + (8 * s + w) * 1056);
      dma16(r1, vo, wb + (ks * 32 + 8) * 3584, L + L_W1 + (8 * s + 4 + w) * 1056);
      ks = ks == 13 ? 0 : ks + 1;
    }
  }
  u32 phq[16];
  {
    f32x16 aq = {};
#pragma unroll
    for (int k = 0; k < 28; k++) aq = mfma32(lds128(L + L_WQQ + k * 1024 + l16), xf[k], aq);
    aq = mfma32(lds128(L + L_AUG + 1024 + (lane & 31) * 16), baug, aq);
    f32x16 p = {};
    p = mfma32(aphi[0], u32x4{pk(aq[0], aq[1]), pk(aq[2], aq[3]), pk(aq[4], aq[5]), pk(aq[6], aq[7])}, p);
    p = mfma32(aphi[1], u32x4{pk(aq[8], aq[9]), pk(aq[10], aq[11]), pk(aq[12], aq[13]), pk(aq[14], aq[15])}, p);
    featmap(p, asc, hi, phq);
  }
  STAMP(9);
  const u32 l16m = (lane & 31) < 24 ? l16 + 1024 : OOB;
  u32x4 wof[8][2];
  poll(ws, WS_F0 + (j8 * 32 + 28 + w) * 128 + (lane & 3) * 8, tag, dead);
#pragma unroll
  for (int s = 0; s < 8; s++) { const u32 so = WS_WO + (j8 * 32 + 8 * w + s) * 2048; wof[s][0] = ld16(rws, l16, so); wof[s][1] = ld16(rws, l16m, so); }
  const int etok = 8 * w + (lane >> 3), ec = lane & 7;
  const u32 evo = ec < 7 ? etok * 896 + 112 * j8 + ec * 16 : OOB;
  u32x4 xres[2];
  xres[0] = ld16p(rx, evo, t32a * 32 * 896); xres[1] = ld16p(rx, evo, (t32a + 4) * 32 * 896);
  const u32x4 b2v = ld16p(mkrsrc(b2, 896), ec < 7 ? 112 * j8 + ec * 16 : OOB, 0);
  const float b1v = tid < 112 ? bflo(b1[112 * j16 + tid]) : 0.f;
  SB();
  poll(ws, WS_FA + (h * 16 + (lane & 15)) * 128 + w * 8, tag, dead);
  STAMP(10);
  {
    u32x4 pt[16];
#pragma unroll
    for (int k = 0; k < 16; k++) pt[k] = ld16(rws, l16, WS_KVP + ((h * 16 + ((k + i) & 15)) * 4 + w) * 1024);
    SB();
    float s[8];
#pragma unroll
    for (int e = 0; e < 4; e++) { s[2 * e] = bflo(pt[0][e]); s[2 * e + 1] = bfhi(pt[0][e]); }
#pragma unroll
    for (int k = 1; k < 16; k++)
#pragma unroll
      for (int e = 0; e < 4; e++) { s[2 * e] = dot2(pt[k][e], SELL, s[2 * e]); s[2 * e + 1] = dot2(pt[k][e], SELH, s[2 * e + 1]); }
    sts128(L + L_KVS + w * 1024 + l16, u32x4{pk(s[0], s[1]), pk(s[2], s[3]), pk(s[4], s[5]), pk(s[6], s[7])});
    if (tid < 112) *(LAS float*)(L + L_TAB + 512 + tid * 4) = b1v;
    bar();
    f32x16 o = {};
#pragma unroll
    for (int st = 0; st < 4; st++) o = mfma32(lds128(L + L_KVS + st * 1024 + l16), u32x4{phq[4 * st], phq[4 * st + 1], phq[4 * st + 2], phq[4 * st + 3]}, o);
    const float inv = __builtin_amdgcn_rcpf(halves(o[12]).y + 1e-6f);
    const u32 so = WS_O + ((4 * i + w) * 32 + 2 * h) * 1024;
    st16(u32x4{pk(o[0] * inv, o[1] * inv), pk(o[2] * inv, o[3] * inv), pk(o[4] * inv, o[5] * inv), pk(o[6] * inv, o[7] * inv)}, rws, l16, so);
    st16(u32x4{pk(o[8] * inv, o[9] * inv), pk(o[10] * inv, o[11] * inv), pk(o[12] * inv, o[13] * inv), pk(o[14] * inv, o[15] * inv)}, rws, l16 + 1024, so);
    publish(ws, WS_FB + (i * 16 + h) * 128 + w * 8, tag, lane);
  }
  STAMP(11);
  u32x4 w2f[28][2];
  poll(ws, WS_F0 + (j8 * 32 + 7 * w + (lane >> 1) % 7) * 128 + 16 + (lane & 1) * 8, tag, dead);
  u32 g4[7];
#pragma unroll
  for (int g = 0; g < 7; g++) { int c = g + rot7; if (c >= 7) c -= 7; g4[g] = 28 * w + 4 * c; }
#pragma unroll
  for (int s = 0; s < 14; s++) {
    const u32 so = WS_W2 + (j8 * 112 + g4[s >> 2] + (s & 2)) * 2048;
    w2f[s][0] = ld16(rws, l16 + (s & 1) * 2048, so); w2f[s][1] = ld16(rws, l16m + (s & 1) * 2048, so);
  }
  SB();
  const u32 a1b = L + L_W1 + (4 * ((lane >> 4) & 1) + ((lane >> 2) & 3)) * 1056 + (lane >> 5) * 256 + (lane & 3) * 8 + 32 * (j16 & 1);
  {
    f32x4 c0v = {}, c1v = {};
    const u32 ab = a1b + w * 32;
    const u32x4 ones = {ONE2, ONE2, ONE2, ONE2};
#pragma unroll 2
    for (int ks = 0; ks < 14; ks++) {
      c0v = mfma16(ldtr2(ab + ks * 8448, ab + ks * 8448 + 512), ones, c0v);
      c1v = mfma16(ldtr2(ab + ks * 8448 + 128, ab + ks * 8448 + 640), ones, c1v);
    }
    if ((lane & 15) == 0) {
      sts128f(L + L_TAB + w * 64 + (lane >> 4) * 16, c0v);
      sts128f(L + L_TAB + 256 + w * 64 + (lane >> 4) * 16, c1v);
    }
  }
  STAMP(12);

  u32 hres[2][4];
  const u32 redw = L + L_RED + w * 8192 + (lane >> 5) * 4096 + (lane & 31) * 128;
  const u32 redr = L + L_RED + etok * 128 + ((ec ^ (etok & 7)) * 16);
  auto red_write = [&](const f32x16& a0, const f32x16& a1) {
#pragma unroll
    for (int g = 0; g < 4; g++) sts128f(redw + ((g ^ (lane & 7)) * 16), f32x4{a0[4 * g], a0[4 * g + 1], a0[4 * g + 2], a0[4 * g + 3]});
#pragma unroll
    for (int g = 0; g < 3; g++) sts128f(redw + (((4 + g) ^ (lane & 7)) * 16), f32x4{a1[4 * g], a1[4 * g + 1], a1[4 * g + 2], a1[4 * g + 3]});
  };
  auto red_read = [&](f32x4& lo, f32x4& hi4) {
    lo = lds128f(redr) + lds128f(redr + 8192) + (lds128f(redr + 16384) + lds128f(redr + 24576));
    hi4 = lds128f(redr + 4096) + lds128f(redr + 12288) + (lds128f(redr + 20480) + lds128f(redr + 28672));
  };
  {
    const int tb = t32a + 4 * ((lane >> 2) & 1);
    poll(ws, WS_FB + ((tb >> 2) * 16 + 4 * w + (lane & 3)) * 128 + (tb & 3) * 8, tag, dead);
    STAMP(13);
    u32x4 of[2][8];
#pragma unroll
    for (int u = 0; u < 2; u++)
#pragma unroll
      for (int s = 0; s < 8; s++) of[u][s] = ld16(rws, l16 + (s & 3) * 1024, WS_O + ((t32a + 4 * u) * 32 + 8 * w + (s & 4)) * 1024);
    SB();
#pragma unroll
    for (int u = 0; u < 2; u++) {
      const int t32 = t32a + 4 * u;
      f32x16 a0 = {}, a1 = {};
#pragma unroll
      for (int s = 0; s < 8; s++) { a0 = mfma32(wof[s][0], of[u][s], a0); a1 = mfma32(wof[s][1], of[u][s], a1); }
      if (u) bar();
      red_write(a0, a1);
      bar();
      f32x4 lo, hi4; red_read(lo, hi4);
      u32x4 hv;
      hv.x = pk(lo[0] + bflo(xres[u].x), lo[1] + bfhi(xres[u].x)); hv.y = pk(lo[2] + bflo(xres[u].y), lo[3] + bfhi(xres[u].y));
      hv.z = pk(hi4[0] + bflo(xres[u].z), hi4[1] + bfhi(xres[u].z)); hv.w = pk(hi4[2] + bflo(xres[u].w), hi4[3] + bfhi(xres[u].w));
      st16(hv, rws, ec < 7 ? (etok >> 4) * 14336 + ec * 256 + (etok & 15) * 16 : OOB, WS_HF + t32 * 28672 + j8 * 1792);
      float s1 = cadd(0.f, hv);
      float s2 = dot2(hv.x, hv.x, 0.f); s2 = dot2(hv.y, hv.y, s2); s2 = dot2(hv.z, hv.z, s2); s2 = dot2(hv.w, hv.w, s2);
      s1 = ec < 7 ? s1 : 0.f; s2 = ec < 7 ? s2 : 0.f;
      s1 += dpp<0xB1>(s1); s2 += dpp<0xB1>(s2); s1 += dpp<0x4E>(s1); s2 += dpp<0x4E>(s2); s1 += dpp<0x141>(s1); s2 += dpp<0x141>(s2);
      __builtin_amdgcn_raw_buffer_store_b64(u32x2{__float_as_uint(s1), __float_as_uint(s2)}, rws, ec == 0 ? etok * 8 : OOB, WS_ST + (t32 * 8 + j8) * 256, 16);
      publish(ws, WS_FC + (t32 * 8 + j8) * 128 + w * 8, tag, lane);
      hres[u][0] = hv.x; hres[u][1] = hv.y; hres[u][2] = hv.z; hres[u][3] = hv.w;
      STAMP(14 + u);
    }
  }
#pragma unroll
  for (int s = 14; s < 28; s++) {
    const u32 so = WS_W2 + (j8 * 112 + g4[s >> 2] + (s & 2)) * 2048;
    w2f[s][0] = ld16(rws, l16 + (s & 1) * 2048, so); w2f[s][1] = ld16(rws, l16m + (s & 1) * 2048, so);
  }
  SB();

#pragma unroll
  for (int u = 0; u < 2; u++) {
    const int t64 = t64a + 2 * u, t16 = 4 * t64 + w, t32 = t16 >> 1;
    poll(ws, WS_FC + (t32 * 8 + ((lane >> 1) & 7)) * 128 + (2 * (t16 & 1) + (lane & 1)) * 8, tag, dead);
    STAMP(16 + 3 * u);
    const u32 svo = (2 * (lane >> 4)) * 256 + (16 * (t16 & 1) + (lane & 15)) * 8;
    const u32x2 sa = __builtin_amdgcn_raw_buffer_load_b64(rws, svo, WS_ST + t32 * 2048, 16);
    const u32x2 sb = __builtin_amdgcn_raw_buffer_load_b64(rws, svo + 256, WS_ST + t32 * 2048, 16);
    u32x4 hf[14];
#pragma unroll
    for (int s = 0; s < 14; s += 2) {
      int c = s + rot14; if (c >= 14) c -= 14;
      const u32 so = WS_HF + t16 * 14336 + c * 1024;
      hf[s] = ld16(rws, l16, so); hf[s + 1] = ld16(rws, l16 + 1024, so);
    }
    SB();
    f32x4 acc[7];
#pragma unroll
    for (int n = 0; n < 7; n++) acc[n] = f32x4{};
#pragma unroll
    for (int s = 0; s < 14; s++)
#pragma unroll
      for (int n = 0; n < 7; n++) acc[n] = mfma16(ldtr2(a1b + s * 8448 + n * 32, a1b + s * 8448 + n * 32 + 512), hf[s], acc[n]);
    STAMP(17 + 3 * u);
    float s1 = __uint_as_float(sa.x) + __uint_as_float(sb.x), s2 = __uint_as_float(sa.y) + __uint_as_float(sb.y);
    { f32x2 t = rows16(s1); s1 = t.x + t.y; t = rows16(s2); s2 = t.x + t.y; t = halves(s1); s1 = t.x + t.y; t = halves(s2); s2 = t.x + t.y; }
    const float mean = s1 * (1.f / 448.f);
    const float rs = __builtin_amdgcn_rsqf(fmaxf(s2 * (1.f / 448.f) - mean * mean, 0.f) + 1e-5f);
    const float mr = -mean * rs;
    u32 hp[14];
#pragma unroll
    for (int n = 0; n < 7; n++) {
      const f32x4 cs = lds128f(L + L_TAB + n * 64 + (lane >> 4) * 16), bb = lds128f(L + L_TAB + 512 + n * 64 + (lane >> 4) * 16);
      float v[4];
#pragma unroll
      for (int e = 0; e < 4; e++) { const float t = fmaf(acc[n][e], rs, fmaf(cs[e], mr, bb[e])); v[e] = t * fmaxf(t, 0.f); }
      hp[2 * n] = pk(v[0], v[1]); hp[2 * n + 1] = pk(v[2], v[3]);
    }
    const u32 so = WS_H1 + (t32 * 112 + 7 * j16) * 1024 + (t16 & 1) * 256;
    const u32 vo = (lane >> 4) * 512 + (lane & 15) * 16;
#pragma unroll
    for (int p = 0; p < 3; p++) st16(u32x4{hp[4 * p], hp[4 * p + 1], hp[4 * p + 2], hp[4 * p + 3]}, rws, vo, so + p * 2048);
    {
      auto s0 = __builtin_amdgcn_permlane16_swap(hp[12], hp[12], false, false);
      auto s1x = __builtin_amdgcn_permlane16_swap(hp[13], hp[13], false, false);
      st16(u32x4{s0[0], s1x[0], s0[1], s1x[1]}, rws, (lane & 16) ? OOB : (lane >> 5) * 512 + (lane & 15) * 16, so + 6144);
    }
    publish(ws, WS_FD + (t64 * 16 + j16) * 128 + w * 8, tag, lane);
    STAMP(18 + 3 * u);
  }

  const rsrc_t rout = mkrsrc(out, 2048 * 896);
#pragma unroll
  for (int u = 0; u < 2; u++) {
    const int t32 = t32a + 4 * u;
    poll(ws, WS_FD + ((t32 >> 1) * 16 + 4 * w + ((lane >> 1) & 3)) * 128 + (2 * (t32 & 1) + (lane & 1)) * 8, tag, dead);
    STAMP(22 + 3 * u);
    u32x4 hf[28];
#pragma unroll
    for (int s = 0; s < 28; s++) hf[s] = ld16(rws, l16 + (s & 3) * 1024, WS_H1 + (t32 * 112 + g4[s >> 2]) * 1024);
    SB();
    f32x16 a0 = {}, a1 = {};
#pragma unroll
    for (int s = 0; s < 28; s++) { a0 = mfma32(w2f[s][0], hf[s], a0); a1 = mfma32(w2f[s][1], hf[s], a1); }
    STAMP(23 + 3 * u);
    bar();
    red_write(a0, a1);
    bar();
    f32x4 lo, hi4; red_read(lo, hi4);
    u32x4 ov;
    ov.x = pk(lo[0] + bflo(b2v.x) + bflo(hres[u][0]), lo[1] + bfhi(b2v.x) + bfhi(hres[u][0]));
    ov.y = pk(lo[2] + bflo(b2v.y) + bflo(hres[u][1]), lo[3] + bfhi(b2v.y) + bfhi(hres[u][1]));
    ov.z = pk(hi4[0] + bflo(b2v.z) + bflo(hres[u][2]), hi4[1] + bfhi(b2v.z) + bfhi(hres[u][2]));
    ov.w = pk(hi4[2] + bflo(b2v.w) + bflo(hres[u][3]), hi4[3] + bfhi(b2v.w) + bfhi(hres[u][3]));
    st16p(ov, rout, evo, t32 * 32 * 896);
    STAMP(24 + 3 * u);
  }
  if (b == 0 && tid == 0) __hip_atomic_store((GAS u64*)(ws + WS_EPOCH), tag, __ATOMIC_RELAXED, __HIP_MEMORY_SCOPE_AGENT);
}

extern "C" void kernel_launch(void* const* d_in, const int* in_sizes, int n_in, void* d_out, int out_size, void* d_ws, size_t ws_size, hipStream_t stream) {
  (void)hipFuncSetAttribute((const void*)fused_block, hipFuncAttributeMaxDynamicSharedMemorySize, L_TOTAL);
  fused_block<<<256, 256, L_TOTAL, stream>>>((const u16*)d_in[0], (const u16*)d_in[1], (const u16*)d_in[2], (const u16*)d_in[3], (const u16*)d_in[5],
                                             (const u16*)d_in[6], (const u16*)d_in[7], (const u16*)d_in[8], (u16*)d_out, (char*)d_ws);
}
```

```cpp
#include <hip/hip_runtime.h>
constexpr unsigned WS_KVP = 466944, WS_WO = 1515520, WS_W2 = 1974272, WS_O = 3579904, WS_HF = 5677056, WS_H1 = 7512064, WS_END = 14852096;
__global__ __launch_bounds__(256, 1) void fused_block(const void* p_x, unsigned n_x, unsigned f_x, const void* p_wqkv, unsigned n_wqkv, unsigned f_wqkv, const void* p_ws, unsigned n_ws, unsigned f_ws, const void* p_w2, unsigned n_w2, unsigned f_w2, const void* p_wo, unsigned n_wo, unsigned f_wo, const void* p_wphi, unsigned n_wphi, unsigned f_wphi, const void* p_w1, unsigned n_w1, unsigned f_w1, const void* p_b1, unsigned n_b1, unsigned f_b1, const void* p_b2, unsigned n_b2, unsigned f_b2, const void* p_out, unsigned n_out, unsigned f_out) {
  asm volatile(
  ".Lk_start:\n"
  "s_load_dwordx16 s[4:19], %0, 0\n"
  "s_load_dwordx16 s[20:35], %0, 64\n"
  "s_load_dwordx8 s[36:43], %0, 128\n"
  "s_getpc_b64 s[50:51]\n"
  ".Lpc0:\n"
  "v_readfirstlane_b32 s52, %2\n"
  "v_and_b32 v1, 63, %2\n"
  "s_lshr_b32 s58, %1, 3\n"
  "s_lshr_b32 s52, s52, 6\n"
  "s_lshl_b32 s97, s58, 2\n"
  "s_add_u32 s97, s97, s52\n"
  "s_and_b32 s59, s97, 15\n"
  "s_lshl_b32 s59, s59, 11\n"
  "v_and_b32 v5, 15, v1\n"
  "v_lshl_add_u32 v5, v5, 7, s59\n"
  "v_min_u32 v5, .Lk_end-.Lpc0-4, v5\n"
  "global_load_dword v15, v5, s[50:51]\n"
  "s_and_b32 s98, %1, 1\n"
  "s_bfe_u32 s59, %1, 0x20001\n"
  "s_lshl_b32 s59, s59, 2\n"
  "s_and_b32 s60, s58, 3\n"
  "s_add_u32 s53, s59, s60\n"
  "s_lshr_b32 s61, s58, 2\n"
  "s_lshl_b32 s59, s98, 3\n"
  "s_add_u32 s54, s59, s61\n"
  "s_lshl_b32 s60, s60, 1\n"
  "s_add_u32 s60, s60, s61\n"
  "s_sub_u32 s59, s60, 7\n"
  "s_min_u32 s55, s59, s60\n"
  "s_mov_b32 s56, s55\n"
  "s_mov_b32 s77, s55\n"
  "s_lshl_b32 s59, s53, 2\n"
  "s_add_u32 s59, s59, s52\n"
  "s_mul_i32 s68, s59, 28672\n"
  "s_and_b32 s61, s54, 1\n"
  "s_lshl_b32 s61, s61, 3\n"
  "s_mul_i32 s59, s52, 43008\n"
  "s_mul_i32 s60, s54, 56\n"
  "s_add_u32 s59, s59, s60\n"
  "s_sub_u32 s59, s59, s61\n"
  "s_add_u32 s69, s59, 896\n"
  "s_mul_i32 s73, s52, 25216\n"
  "s_lshl_b32 s59, s52, 11\n"
  "s_add_u32 s87, s59, 100864\n"
  "v_lshlrev_b32 v2, 4, v1\n"
  "v_lshrrev_b32 v5, 3, v1\n"
  "v_mul_u32_u24 v5, 896, v5\n"
  "v_lshrrev_b32 v7, 4, v1\n"
  "v_and_b32 v106, 7, v1\n"
  "v_xor_b32 v7, v7, v106\n"
  "v_lshl_add_u32 v88, v7, 4, v5\n"
  "v_lshl_add_u32 v114, v106, 4, v5\n"
  "v_xor_b32 v7, 4, v7\n"
  "v_lshl_add_u32 v89, v7, 4, v5\n"
  "v_add_u32 v89, 6144, v89\n"
  "v_add_u32 v90, 12288, v88\n"
  "v_add_u32 v91, 12288, v89\n"
  "v_bfe_u32 v5, v1, 2, 2\n"
  "v_bfe_u32 v7, v1, 4, 1\n"
  "v_lshl_or_b32 v107, v7, 3, v5\n"
  "v_lshrrev_b32 v106, 5, v1\n"
  "v_lshl_or_b32 v107, v106, 2, v107\n"
  "v_and_b32 v104, 3, v1\n"
  "v_lshlrev_b32 v105, 4, v104\n"
  "v_mul_u32_u24 v92, 2688, v107\n"
  "v_add_u32 v92, v92, v105\n"
  "s_lshl_b32 s58, %1, 2\n"
  "s_add_u32 s58, s58, s52\n"
  "s_mul_i32 s59, s58, 9363\n"
  "s_lshr_b32 s59, s59, 20\n"
  "s_mul_i32 s60, s59, 112\n"
  "s_sub_u32 s60, s58, s60\n"
  "s_mul_i32 s60, s60, 14336\n"
  "s_lshl_b32 s59, s59, 7\n"
  "s_add_u32 s86, s59, s60\n"
  "s_lshl_b32 s59, s58, 11\n"
  "s_add_u32 s85, s59, 1974272\n"
  "s_sub_u32 s59, s58, 784\n"
  "s_bfe_u32 s60, s59, 0x40001\n"
  "s_mul_i32 s60, s60, 25088\n"
  "s_and_b32 s61, s59, 1\n"
  "s_mul_i32 s61, s61, 12544\n"
  "s_add_u32 s60, s60, s61\n"
  "s_lshr_b32 s61, s59, 5\n"
  "s_lshl_b32 s61, s61, 7\n"
  "s_add_u32 s60, s60, s61\n"
  "s_lshl_b32 s59, s59, 11\n"
  "s_add_u32 s59, s59, 1515520\n"
  "s_cmp_lt_u32 s58, 1008\n"
  "s_cselect_b32 s60, s60, 1073741824\n"
  "s_cselect_b32 s59, s59, 1073741824\n"
  "s_cmp_lt_u32 s58, 784\n"
  "s_cselect_b32 s86, s86, s60\n"
  "s_cselect_b32 s85, s85, s59\n"
  "s_cselect_b32 s99, 1, 0\n"
  "s_waitcnt lgkmcnt(0)\n"
  "s_lshl_b32 s58, s56, 7\n"
  "s_add_u32 s58, s58, s68\n"
  "s_add_u32 s56, s56, 1\n"
  "s_cmp_eq_u32 s56, 7\n"
  "s_cselect_b32 s56, 0, s56\n"
  "s_add_u32 m0, s73, 4096\n"
  "s_nop 0\n"
  "buffer_load_dwordx4 v88, s[4:7], s58 offen offset:0 lds\n"
  "buffer_load_dwordx4 v89, s[4:7], s58 offen offset:1024 lds\n"
  "buffer_load_dwordx4 v90, s[4:7], s58 offen offset:2048 lds\n"
  "buffer_load_dwordx4 v91, s[4:7], s58 offen offset:3072 lds\n"
  "s_mul_i32 s59, s77, 172032\n"
  "s_add_u32 m0, s87, 1024\n"
  "s_add_u32 s59, s59, s69\n"
  "buffer_load_dwordx4 v92, s[8:11], s59 offen offset:0 lds\n"
  "s_add_u32 m0, s87, 0\n"
  "s_add_u32 s59, s59, 896\n"
  "buffer_load_dwordx4 v92, s[8:11], s59 offen offset:0 lds\n"
  "s_add_u32 s77, s77, 1\n"
  "s_cmp_eq_u32 s77, 7\n"
  "s_cselect_b32 s77, 0, s77\n"
  "s_load_dwordx2 s[44:45], s[12:13], 0\n"
  "s_mul_i32 s59, s77, 172032\n"
  "s_add_u32 m0, s87, 9232\n"
  "s_add_u32 s59, s59, s69\n"
  "buffer_load_dwordx4 v92, s[8:11], s59 offen offset:0 lds\n"
  "s_add_u32 m0, s87, 8208\n"
  "s_add_u32 s59, s59, 896\n"
  "buffer_load_dwordx4 v92, s[8:11], s59 offen offset:0 lds\n"
  "s_add_u32 s77, s77, 1\n"
  "s_cmp_eq_u32 s77, 7\n"
  "s_cselect_b32 s77, 0, s77\n"
  "s_lshl_b32 s58, s56, 7\n"
  "s_add_u32 s58, s58, s68\n"
  "s_add_u32 s56, s56, 1\n"
  "s_cmp_eq_u32 s56, 7\n"
  "s_cselect_b32 s56, 0, s56\n"
  "s_add_u32 m0, s73, 8192\n"
  "s_nop 0\n"
  "buffer_load_dwordx4 v88, s[4:7], s58 offen offset:0 lds\n"
  "buffer_load_dwordx4 v89, s[4:7], s58 offen offset:1024 lds\n"
  "buffer_load_dwordx4 v90, s[4:7], s58 offen offset:2048 lds\n"
  "buffer_load_dwordx4 v91, s[4:7], s58 offen offset:3072 lds\n"
  "s_cmp_eq_u32 s99, 1\n"
  "s_cselect_b64 s[16:17], s[16:17], s[20:21]\n"
  "s_cselect_b64 s[18:19], s[18:19], s[22:23]\n"
  "s_cselect_b64 vcc, -1, 0\n"
  "v_lshrrev_b32 v5, 5, v1\n"
  "v_mul_u32_u24 v5, 896, v5\n"
  "v_sub_u32 v5, v114, v5\n"
  "v_cndmask_b32 v113, v5, v114, vcc\n"
  "s_or_b32 vcc_lo, vcc_lo, 0x00ffffff\n"
  "s_or_b32 vcc_hi, vcc_hi, 0x00ffffff\n"
  "v_mov_b32 v5, 0x7f000000\n"
  "v_cndmask_b32 v113, v5, v113, vcc\n"
  "v_add_u32 v113, 6144, v113\n"
  "s_add_u32 m0, s73, 21504\n"
  "s_nop 0\n"
  "buffer_load_dwordx4 v114, s[16:19], s86 offen offset:0 lds\n"
  "s_add_u32 m0, s73, 21504\n"
  "s_nop 0\n"
  "buffer_load_dwordx4 v113, s[16:19], s86 offen offset:1024 lds\n"
  "s_mul_i32 s58, s54, 1568\n"
  "s_lshl_b32 s59, s52, 10\n"
  "s_add_u32 s58, s58, s59\n"
  "s_add_u32 m0, s59, 158320\n"
  "s_cmp_lt_u32 s52, 2\n"
  "s_cbranch_scc0 .Lnophi\n"
  "buffer_load_dwordx4 v2, s[24:27], s58 offen offset:0 lds\n"
  ".Lnophi:\n"
  "s_mov_b32 s63, 8208\n"
  "s_mov_b32 s70, 0x3f803f80\n"
  "s_mov_b32 s71, 0x3f80\n"
  "s_mov_b32 s72, 0x3f800000\n"
  "s_mov_b32 s74, 0\n"
  "s_mov_b32 s75, -1\n"
  "s_mov_b64 s[48:49], -1\n"
  "s_mov_b32 s76, 100000\n"
  "v_lshrrev_b32 v106, 5, v1\n"
  "v_bfe_u32 v5, v1, 2, 2\n"
  "v_bfe_u32 v7, v1, 4, 1\n"
  "v_lshlrev_b32 v7, 5, v7\n"
  "v_lshl_or_b32 v7, v104, 3, v7\n"
  "v_lshl_or_b32 v6, v106, 2, v5\n"
  "v_lshl_or_b32 v3, v6, 6, v7\n"
  "v_lshl_or_b32 v93, v6, 7, v7\n"
  "s_add_u32 s58, s73, 21504\n"
  "v_add_u32 v93, s58, v93\n"
  "s_and_b32 s61, s54, 1\n"
  "s_lshl_b32 s61, s61, 3\n"
  "v_and_b32 v5, 31, v1\n"
  "v_lshlrev_b32 v7, 7, v5\n"
  "v_add_u32 v7, s73, v7\n"
  "v_add_u32 v6, 20, v5\n"
  "v_lshrrev_b32 v6, 5, v6\n"
  "v_sub_u32 v108, v5, v6\n"
  "v_lshrrev_b32 v6, 4, v5\n"
  "v_sub_u32 v108, v108, v6\n"
  "v_add_u32 v6, 4, v5\n"
  "v_lshrrev_b32 v6, 5, v6\n"
  "v_sub_u32 v108, v108, v6\n"
  "v_mul_u32_u24 v6, 7168, v106\n"
  "v_lshl_add_u32 v14, v5, 4, v6\n"
  "v_bfe_u32 v5, v1, 1, 3\n"
  "v_xor_b32 v5, v5, v106\n"
  "s_add_u32 s58, s52, 0\n"
  "s_and_b32 s58, s58, 3\n"
  "s_lshl_b32 s59, s58, 1\n"
  "v_xor_b32 v94, s59, v5\n"
  "v_lshl_add_u32 v94, v94, 4, v7\n"
  "s_lshl_b32 s59, s58, 11\n"
  "s_add_u32 s59, s59, s61\n"
  "s_add_u32 s59, s59, 100864\n"
  "v_add_u32 v98, s59, v3\n"
  "s_add_u32 s58, s52, 1\n"
  "s_and_b32 s58, s58, 3\n"
  "s_lshl_b32 s59, s58, 1\n"
  "v_xor_b32 v95, s59, v5\n"
  "v_lshl_add_u32 v95, v95, 4, v7\n"
  "s_lshl_b32 s59, s58, 11\n"
  "s_add_u32 s59, s59, s61\n"
  "s_add_u32 s59, s59, 100864\n"
  "v_add_u32 v99, s59, v3\n"
  "s_add_u32 s58, s52, 2\n"
  "s_and_b32 s58, s58, 3\n"
  "s_lshl_b32 s59, s58, 1\n"
  "v_xor_b32 v96, s59, v5\n"
  "v_lshl_add_u32 v96, v96, 4, v7\n"
  "s_lshl_b32 s59, s58, 11\n"
  "s_add_u32 s59, s59, s61\n"
  "s_add_u32 s59, s59, 100864\n"
  "v_add_u32 v100, s59, v3\n"
  "s_add_u32 s58, s52, 3\n"
  "s_and_b32 s58, s58, 3\n"
  "s_lshl_b32 s59, s58, 1\n"
  "v_xor_b32 v97, s59, v5\n"
  "v_lshl_add_u32 v97, v97, 4, v7\n"
  "s_lshl_b32 s59, s58, 11\n"
  "s_add_u32 s59, s59, s61\n"
  "s_add_u32 s59, s59, 100864\n"
  "v_add_u32 v101, s59, v3\n"
  "v_and_b32 v5, 0x13, v1\n"
  "v_cmp_eq_u32 vcc, 0x13, v5\n"
  "v_mov_b32 v5, 109056\n"
  "v_add_u32 v124, 512, v98\n"
  "v_cndmask_b32 v80, v98, v5, vcc\n"
  "v_cndmask_b32 v124, v124, v5, vcc\n"
  "v_add_u32 v125, 512, v99\n"
  "v_cndmask_b32 v81, v99, v5, vcc\n"
  "v_cndmask_b32 v125, v125, v5, vcc\n"
  "v_add_u32 v126, 512, v100\n"
  "v_cndmask_b32 v82, v100, v5, vcc\n"
  "v_cndmask_b32 v126, v126, v5, vcc\n"
  "v_add_u32 v127, 512, v101\n"
  "v_cndmask_b32 v83, v101, v5, vcc\n"
  "v_cndmask_b32 v127, v127, v5, vcc\n"
  "v_mov_b32 v104, 0x3f80\n"
  "v_mov_b32 v105, 0\n"
  "v_min_u32 v6, 6, v1\n"
  "v_mad_u32_u24 v6, v6, s63, v5\n"
  "ds_write_b64 v6, v[104:105]\n"
  "v_accvgpr_write_b32 a80, -1\n"
  "v_mov_b32 v84, s70\n"
  "v_accvgpr_write_b32 a81, -1\n"
  "v_mov_b32 v85, s70\n"
  "v_accvgpr_write_b32 a82, -1\n"
  "v_mov_b32 v86, s70\n"
  "v_accvgpr_write_b32 a83, -1\n"
  "v_mov_b32 v87, s70\n"
  "v_min_u32 v6, 27, v108\n"
  "v_mul_u32_u24 v110, 224, v106\n"
  "v_lshl_add_u32 v110, v6, 1, v110\n"
  "v_add_u32 v110, 158320, v110\n"
  "v_mul_u32_u24 v5, 37, v1\n"
  "v_lshrrev_b32 v5, 9, v5\n"
  "v_mul_u32_u24 v6, 14, v5\n"
  "v_sub_u32 v6, v1, v6\n"
  "v_mul_u32_u24 v103, 3584, v5\n"
  "v_lshl_add_u32 v103, v6, 7, v103\n"
  "v_and_b32 v7, 1, v5\n"
  "v_lshlrev_b32 v7, 2, v7\n"
  "v_lshrrev_b32 v5, 1, v5\n"
  "v_add_u32 v5, v5, v7\n"
  "v_mul_u32_u24 v5, 3584, v5\n"
  "v_lshl_add_u32 v5, v6, 4, v5\n"
  "v_cmp_gt_u32 vcc, 56, v1\n"
  "v_mov_b32 v6, 0x7f000000\n"
  "s_nop 0\n"
  "v_cndmask_b32 v8, v6, v5, vcc\n"
  "v_add_u32 v9, 7168, v8\n"
  "v_add_u32 v10, 28672, v8\n"
  "v_add_u32 v11, 35840, v8\n"
  "s_mul_i32 s58, s52, 57344\n"
  "s_mul_i32 s59, s54, 224\n"
  "s_add_u32 s88, s58, s59\n"
  "s_mov_b32 s90, s55\n"
  "s_lshr_b32 s58, %1, 3\n"
  "s_mul_i32 s59, s58, 37\n"
  "s_lshr_b32 s59, s59, 8\n"
  "s_mul_i32 s60, s59, 7\n"
  "s_sub_u32 s84, s58, s60\n"
  "s_add_u32 s57, s59, s84\n"
  "s_and_b32 s60, %1, 7\n"
  "s_lshl_b32 s60, s60, 2\n"
  "s_add_u32 s60, s60, s59\n"
  "s_lshl_b32 s83, s60, 1\n"
  "s_cmp_lt_u32 s58, 28\n"
  "s_cselect_b32 s3, 0, 1073741824\n"
  "s_lshl_b32 s58, s54, 2\n"
  "s_add_u32 s58, s58, s52\n"
  "s_lshl_b32 s58, s58, 4\n"
  "s_add_u32 s58, s58, s53\n"
  "s_lshl_b32 s58, s58, 10\n"
  "s_add_u32 s78, s58, 466944\n"
  "s_lshl_b32 s58, s53, 2\n"
  "s_add_u32 s58, s58, s52\n"
  "s_lshl_b32 s59, s58, 4\n"
  "s_add_u32 s59, s59, s54\n"
  "s_lshl_b32 s59, s59, 11\n"
  "s_add_u32 s79, s59, 3579904\n"
  "s_mul_i32 s59, s58, 112\n"
  "s_mul_i32 s60, s54, 7\n"
  "s_add_u32 s59, s59, s60\n"
  "s_lshl_b32 s59, s59, 10\n"
  "s_add_u32 s82, s59, 7512064\n"
  "s_mul_i32 s58, s83, 28\n"
  "s_lshl_b32 s59, s84, 2\n"
  "s_add_u32 s59, s59, s52\n"
  "s_add_u32 s58, s58, s59\n"
  "s_lshl_b32 s58, s58, 10\n"
  "s_add_u32 s58, s58, 5677056\n"
  "s_add_u32 s80, s58, s3\n"
  "s_lshr_b32 s60, s59, 1\n"
  "s_lshl_b32 s60, s60, 9\n"
  "s_and_b32 s59, s59, 1\n"
  "s_lshl_b32 s59, s59, 3\n"
  "s_add_u32 s59, s59, s60\n"
  "s_mul_i32 s58, s83, 7168\n"
  "s_add_u32 s58, s58, s59\n"
  "s_add_u32 s58, s58, 8192\n"
  "s_add_u32 s81, s58, s3\n"
  "s_and_b32 s58, s52, 1\n"
  "s_lshl_b32 s58, s58, 8\n"
  "s_lshr_b32 s59, s52, 1\n"
  "s_lshl_b32 s59, s59, 10\n"
  "s_or_b32 s58, s58, s59\n"
  "v_xor_b32 v12, s58, v2\n"
  "v_xor_b32 v13, 0x400, v12\n"
  "s_mul_i32 s59, s77, 172032\n"
  "s_add_u32 m0, s87, 17440\n"
  "s_add_u32 s59, s59, s69\n"
  "buffer_load_dwordx4 v92, s[8:11], s59 offen offset:0 lds\n"
  "s_add_u32 m0, s87, 16416\n"
  "s_add_u32 s59, s59, 896\n"
  "buffer_load_dwordx4 v92, s[8:11], s59 offen offset:0 lds\n"
  "s_add_u32 s77, s77, 1\n"
  "s_cmp_eq_u32 s77, 7\n"
  "s_cselect_b32 s77, 0, s77\n"
  "s_lshl_b32 s58, s56, 7\n"
  "s_add_u32 s58, s58, s68\n"
  "s_add_u32 s56, s56, 1\n"
  "s_cmp_eq_u32 s56, 7\n"
  "s_cselect_b32 s56, 0, s56\n"
  "s_add_u32 m0, s73, 12288\n"
  "s_nop 0\n"
  "buffer_load_dwordx4 v88, s[4:7], s58 offen offset:0 lds\n"
  "buffer_load_dwordx4 v89, s[4:7], s58 offen offset:1024 lds\n"
  "buffer_load_dwordx4 v90, s[4:7], s58 offen offset:2048 lds\n"
  "buffer_load_dwordx4 v91, s[4:7], s58 offen offset:3072 lds\n"
  "s_waitcnt lgkmcnt(0)\n"
  "s_and_b32 s61, s44, 0xff\n"
  "s_sub_u32 s62, s61, 254\n"
  "s_min_u32 s61, s61, s62\n"
  "s_add_u32 s61, s61, 1\n"
  "s_mul_hi_u32 s58, s44, 0x4c957f2d\n"
  "s_mul_i32 s59, s44, 0x5851f42d\n"
  "s_mul_i32 s60, s45, 0x4c957f2d\n"
  "s_mul_i32 s44, s44, 0x4c957f2d\n"
  "s_add_u32 s58, s58, s59\n"
  "s_add_u32 s58, s58, s60\n"
  "s_add_u32 s44, s44, 0xf767814f\n"
  "s_addc_u32 s45, s58, 0x14057b7e\n"
  "s_and_b32 s44, s44, 0xffffff00\n"
  "s_or_b32 s44, s44, s61\n"
  "s_and_b32 s58, s61, 1\n"
  "s_sub_u32 s58, 0x3f, s58\n"
  "s_lshl_b32 s58, s58, 8\n"
  "s_or_b32 s58, s58, s61\n"
  "s_lshl_b32 s100, s58, 16\n"
  "s_lshl_b32 s58, s61, 15\n"
  "s_or_b32 s58, s58, s61\n"
  "s_lshl_b32 s59, s58, 15\n"
  "s_and_b32 s50, s59, 0x80008000\n"
  "s_lshl_b32 s59, s58, 13\n"
  "s_and_b32 s51, s59, 0x80008000\n"
  "s_lshl_b32 s59, s58, 11\n"
  "s_and_b32 s93, s59, 0x80008000\n"
  "s_lshl_b32 s59, s58, 9\n"
  "s_and_b32 s101, s59, 0x80008000\n"
  "s_cmp_eq_u32 s99, 1\n"
  "s_cselect_b32 s20, s50, 0\n"
  "s_cselect_b32 s21, s51, 0\n"
  "s_cselect_b32 s22, s93, 0\n"
  "s_cselect_b32 s23, s101, 0\n"
  "s_waitcnt vmcnt(14)\n"
  "s_barrier\n"
  "ds_read_b64_tr_b16 a[0:1], v80 offset:0\n"
  "ds_read_b64_tr_b16 a[2:3], v124 offset:0\n"
  "ds_read_b64_tr_b16 a[4:5], v98 offset:1024\n"
  "ds_read_b64_tr_b16 a[6:7], v98 offset:1536\n"
  "ds_read_b128 v[128:131], v94 offset:4096\n"
  "ds_read_b128 v[132:135], v95 offset:4096\n"
  "ds_read_b128 v[136:139], v96 offset:4096\n"
  "ds_read_b128 v[140:143], v97 offset:4096\n"
  "ds_read_b64_tr_b16 a[16:17], v81 offset:0\n"
  "ds_read_b64_tr_b16 a[18:19], v125 offset:0\n"
  "ds_read_b64_tr_b16 a[20:21], v99 offset:1024\n"
  "ds_read_b64_tr_b16 a[22:23], v99 offset:1536\n"
  "ds_read_b64_tr_b16 a[24:25], v82 offset:0\n"
  "ds_read_b64_tr_b16 a[26:27], v126 offset:0\n"
  "ds_read_b64_tr_b16 a[28:29], v100 offset:1024\n"
  "ds_read_b64_tr_b16 a[30:31], v100 offset:1536\n"
  "s_waitcnt lgkmcnt(14)\n"
  "v_mfma_f32_32x32x16_bf16 a[48:63], v[84:87], a[0:3], 0\n"
  "ds_read_b64_tr_b16 a[32:33], v83 offset:0\n"
  "ds_read_b64_tr_b16 a[34:35], v127 offset:0\n"
  "ds_read_b64_tr_b16 a[36:37], v101 offset:1024\n"
  "ds_read_b64_tr_b16 a[38:39], v101 offset:1536\n"
  "s_mul_i32 s59, s77, 172032\n"
  "s_add_u32 m0, s87, 25648\n"
  "s_add_u32 s59, s59, s69\n"
  "buffer_load_dwordx4 v92, s[8:11], s59 offen offset:0 lds\n"
  "s_waitcnt lgkmcnt(15)\n"
  "v_mfma_f32_32x32x16_bf16 a[64:79], v[84:87], a[4:7], 0\n"
  "s_add_u32 m0, s87, 24624\n"
  "s_add_u32 s59, s59, 896\n"
  "buffer_load_dwordx4 v92, s[8:11], s59 offen offset:0 lds\n"
  "s_add_u32 s77, s77, 1\n"
  "s_cmp_eq_u32 s77, 7\n"
  "s_cselect_b32 s77, 0, s77\n"
  "v_mfma_f32_32x32x16_bf16 v[32:47], a[0:3], v[128:131], 0\n"
  "s_lshl_b32 s58, s56, 7\n"
  "s_add_u32 s58, s58, s68\n"
  "s_add_u32 s56, s56, 1\n"
  "s_cmp_eq_u32 s56, 7\n"
  "s_cselect_b32 s56, 0, s56\n"
  "s_add_u32 m0, s73, 0\n"
  "s_nop 0\n"
  "buffer_load_dwordx4 v88, s[4:7], s58 offen offset:0 lds\n"
  "v_mfma_f32_32x32x16_bf16 v[16:31], a[4:7], v[128:131], 0\n"
  "buffer_load_dwordx4 v89, s[4:7], s58 offen offset:1024 lds\n"
  "v_mfma_f32_32x32x16_bf16 v[48:63], v[128:131], v[128:131], 0\n"
  "buffer_load_dwordx4 v90, s[4:7], s58 offen offset:2048 lds\n"
  "s_waitcnt lgkmcnt(14)\n"
  "v_mfma_f32_32x32x16_bf16 v[48:63], v[132:135], v[132:135], v[48:63]\n"
  "buffer_load_dwordx4 v91, s[4:7], s58 offen offset:3072 lds\n"
  "s_waitcnt lgkmcnt(13)\n"
  "v_mfma_f32_32x32x16_bf16 v[48:63], v[136:139], v[136:139], v[48:63]\n"
  "s_waitcnt lgkmcnt(12)\n"
  "v_mfma_f32_32x32x16_bf16 v[48:63], v[140:143], v[140:143], v[48:63]\n"
  "s_waitcnt lgkmcnt(8)\n"
  "v_mfma_f32_32x32x16_bf16 v[32:47], a[16:19], v[132:135], v[32:47]\n"
  "v_mfma_f32_32x32x16_bf16 v[16:31], a[20:23], v[132:135], v[16:31]\n"
  "s_waitcnt vmcnt(14)\n"
  "s_barrier\n"
  "ds_read_b64_tr_b16 a[8:9], v80 offset:8208\n"
  "ds_read_b64_tr_b16 a[10:11], v124 offset:8208\n"
  "ds_read_b64_tr_b16 a[12:13], v98 offset:9232\n"
  "ds_read_b64_tr_b16 a[14:15], v98 offset:9744\n"
  "s_waitcnt lgkmcnt(8)\n"
  "v_mfma_f32_32x32x16_bf16 v[32:47], a[24:27], v[136:139], v[32:47]\n"
  "ds_read_b128 v[144:147], v94 offset:8192\n"
  "ds_read_b128 v[148:151], v95 offset:8192\n"
  "ds_read_b128 v[152:155], v96 offset:8192\n"
  "ds_read_b128 v[156:159], v97 offset:8192\n"
  "v_mfma_f32_32x32x16_bf16 v[16:31], a[28:31], v[136:139], v[16:31]\n"
  "ds_read_b64_tr_b16 a[16:17], v81 offset:8208\n"
  "ds_read_b64_tr_b16 a[18:19], v125 offset:8208\n"
  "ds_read_b64_tr_b16 a[20:21], v99 offset:9232\n"
  "ds_read_b64_tr_b16 a[22:23], v99 offset:9744\n"
  "s_waitcnt lgkmcnt(12)\n"
  "v_mfma_f32_32x32x16_bf16 v[32:47], a[32:35], v[140:143], v[32:47]\n"
  "ds_read_b64_tr_b16 a[24:25], v82 offset:8208\n"
  "ds_read_b64_tr_b16 a[26:27], v126 offset:8208\n"
  "ds_read_b64_tr_b16 a[28:29], v100 offset:9232\n"
  "ds_read_b64_tr_b16 a[30:31], v100 offset:9744\n"
  "v_mfma_f32_32x32x16_bf16 v[16:31], a[36:39], v[140:143], v[16:31]\n"
  "s_waitcnt lgkmcnt(14)\n"
  "v_mfma_f32_32x32x16_bf16 a[48:63], v[84:87], a[8:11], a[48:63]\n"
  "ds_read_b64_tr_b16 a[32:33], v83 offset:8208\n"
  "ds_read_b64_tr_b16 a[34:35], v127 offset:8208\n"
  "ds_read_b64_tr_b16 a[36:37], v101 offset:9232\n"
  "ds_read_b64_tr_b16 a[38:39], v101 offset:9744\n"
  "s_mul_i32 s59, s77, 172032\n"
  "s_add_u32 m0, s87, 33856\n"
  "s_add_u32 s59, s59, s69\n"
  "buffer_load_dwordx4 v92, s[8:11], s59 offen offset:0 lds\n"
  "s_waitcnt lgkmcnt(15)\n"
  "v_mfma_f32_32x32x16_bf16 a[64:79], v[84:87], a[12:15], a[64:79]\n"
  "s_add_u32 m0, s87, 32832\n"
  "s_add_u32 s59, s59, 896\n"
  "buffer_load_dwordx4 v92, s[8:11], s59 offen offset:0 lds\n"
  "s_add_u32 s77, s77, 1\n"
  "s_cmp_eq_u32 s77, 7\n"
  "s_cselect_b32 s77, 0, s77\n"
  "v_mfma_f32_32x32x16_bf16 v[32:47], a[8:11], v[144:147], v[32:47]\n"
  "s_lshl_b32 s58, s56, 7\n"
  "s_add_u32 s58, s58, s68\n"
  "s_add_u32 s56, s56, 1\n"
  "s_cmp_eq_u32 s56, 7\n"
  "s_cselect_b32 s56, 0, s56\n"
  "s_add_u32 m0, s73, 4096\n"
  "s_nop 0\n"
  "buffer_load_dwordx4 v88, s[4:7], s58 offen offset:0 lds\n"
  "v_mfma_f32_32x32x16_bf16 v[16:31], a[12:15], v[144:147], v[16:31]\n"
  "buffer_load_dwordx4 v89, s[4:7], s58 offen offset:1024 lds\n"
  "v_mfma_f32_32x32x16_bf16 v[48:63], v[144:147], v[144:147], v[48:63]\n"
  "buffer_load_dwordx4 v90, s[4:7], s58 offen offset:2048 lds\n"
  "s_waitcnt lgkmcnt(14)\n"
  "v_mfma_f32_32x32x16_bf16 v[48:63], v[148:151], v[148:151], v[48:63]\n"
  "buffer_load_dwordx4 v91, s[4:7], s58 offen offset:3072 lds\n"
  "s_waitcnt lgkmcnt(13)\n"
  "v_mfma_f32_32x32x16_bf16 v[48:63], v[152:155], v[152:155], v[48:63]\n"
  "s_waitcnt vmcnt(18)\n"
  "ds_read_b64_tr_b16 v[248:249], v93 offset:0\n"
  "ds_read_b64_tr_b16 v[250:251], v93 offset:1024\n"
  "ds_read_b64_tr_b16 v[252:253], v93 offset:64\n"
  "ds_read_b64_tr_b16 v[254:255], v93 offset:1088\n"
  "s_waitcnt lgkmcnt(15)\n"
  "v_mfma_f32_32x32x16_bf16 v[48:63], v[156:159], v[156:159], v[48:63]\n"
  "s_waitcnt lgkmcnt(12)\n"
  "v_mfma_f32_32x32x16_bf16 v[32:47], a[16:19], v[148:151], v[32:47]\n"
  "v_mfma_f32_32x32x16_bf16 v[16:31], a[20:23], v[148:151], v[16:31]\n"
  "s_waitcnt vmcnt(12)\n"
  "s_barrier\n"
  "ds_read_b64_tr_b16 a[0:1], v80 offset:16416\n"
  "ds_read_b64_tr_b16 a[2:3], v124 offset:16416\n"
  "ds_read_b64_tr_b16 a[4:5], v98 offset:17440\n"
  "ds_read_b64_tr_b16 a[6:7], v98 offset:17952\n"
  "s_waitcnt lgkmcnt(12)\n"
  "v_mfma_f32_32x32x16_bf16 v[32:47], a[24:27], v[152:155], v[32:47]\n"
  "ds_read_b128 v[160:163], v94 offset:12288\n"
  "ds_read_b128 v[164:167], v95 offset:12288\n"
  "ds_read_b128 v[168:171], v96 offset:12288\n"
  "ds_read_b128 v[172:175], v97 offset:12288\n"
  "v_mfma_f32_32x32x16_bf16 v[16:31], a[28:31], v[152:155], v[16:31]\n"
  "ds_read_b64_tr_b16 a[16:17], v81 offset:16416\n"
  "ds_read_b64_tr_b16 a[18:19], v125 offset:16416\n"
  "ds_read_b64_tr_b16 a[20:21], v99 offset:17440\n"
  "ds_read_b64_tr_b16 a[22:23], v99 offset:17952\n"
  "s_waitcnt lgkmcnt(15)\n"
  "v_mfma_f32_32x32x16_bf16 v[32:47], a[32:35], v[156:159], v[32:47]\n"
  "ds_read_b64_tr_b16 a[24:25], v82 offset:16416\n"
  "ds_read_b64_tr_b16 a[26:27], v126 offset:16416\n"
  "ds_read_b64_tr_b16 a[28:29], v100 offset:17440\n"
  "ds_read_b64_tr_b16 a[30:31], v100 offset:17952\n"
  "v_mfma_f32_32x32x16_bf16 v[16:31], a[36:39], v[156:159], v[16:31]\n"
  "s_waitcnt lgkmcnt(14)\n"
  "v_mfma_f32_32x32x16_bf16 a[48:63], v[84:87], a[0:3], a[48:63]\n"
  "ds_read_b64_tr_b16 a[32:33], v83 offset:16416\n"
  "ds_read_b64_tr_b16 a[34:35], v127 offset:16416\n"
  "ds_read_b64_tr_b16 a[36:37], v101 offset:17440\n"
  "ds_read_b64_tr_b16 a[38:39], v101 offset:17952\n"
  "s_mul_i32 s59, s77, 172032\n"
  "s_add_u32 m0, s87, 42064\n"
  "s_add_u32 s59, s59, s69\n"
  "buffer_load_dwordx4 v92, s[8:11], s59 offen offset:0 lds\n"
  "s_waitcnt lgkmcnt(15)\n"
  "v_mfma_f32_32x32x16_bf16 a[64:79], v[84:87], a[4:7], a[64:79]\n"
  "s_add_u32 m0, s87, 41040\n"
  "s_add_u32 s59, s59, 896\n"
  "buffer_load_dwordx4 v92, s[8:11], s59 offen offset:0 lds\n"
  "s_add_u32 s77, s77, 1\n"
  "s_cmp_eq_u32 s77, 7\n"
  "s_cselect_b32 s77, 0, s77\n"
  "v_mfma_f32_32x32x16_bf16 v[32:47], a[0:3], v[160:163], v[32:47]\n"
  "s_lshl_b32 s58, s56, 7\n"
  "s_add_u32 s58, s58, s68\n"
  "s_add_u32 s56, s56, 1\n"
  "s_cmp_eq_u32 s56, 7\n"
  "s_cselect_b32 s56, 0, s56\n"
  "s_add_u32 m0, s73, 8192\n"
  "s_nop 0\n"
  "buffer_load_dwordx4 v88, s[4:7], s58 offen offset:0 lds\n"
  "v_mfma_f32_32x32x16_bf16 v[16:31], a[4:7], v[160:163], v[16:31]\n"
  "v_xor_b32 v248, s20, v248\n"
  "v_xor_b32 v249, s21, v249\n"
  "v_xor_b32 v250, s22, v250\n"
  "v_xor_b32 v251, s23, v251\n"
  "buffer_load_dwordx4 v89, s[4:7], s58 offen offset:1024 lds\n"
  "v_mfma_f32_32x32x16_bf16 v[48:63], v[160:163], v[160:163], v[48:63]\n"
  "v_xor_b32 v252, s20, v252\n"
  "v_xor_b32 v253, s21, v253\n"
  "v_xor_b32 v254, s22, v254\n"
  "v_xor_b32 v255, s23, v255\n"
  "buffer_load_dwordx4 v90, s[4:7], s58 offen offset:2048 lds\n"
  "s_waitcnt lgkmcnt(14)\n"
  "v_mfma_f32_32x32x16_bf16 v[48:63], v[164:167], v[164:167], v[48:63]\n"
  "buffer_load_dwordx4 v91, s[4:7], s58 offen offset:3072 lds\n"
  "s_waitcnt lgkmcnt(13)\n"
  "v_mfma_f32_32x32x16_bf16 v[48:63], v[168:171], v[168:171], v[48:63]\n"
  "buffer_store_dwordx4 v[248:251], v2, s[12:15], s85 offen offset:0 sc1\n"
  "s_waitcnt lgkmcnt(12)\n"
  "v_mfma_f32_32x32x16_bf16 v[48:63], v[172:175], v[172:175], v[48:63]\n"
  "buffer_store_dwordx4 v[252:255], v2, s[12:15], s85 offen offset:1024 sc1\n"
  "s_waitcnt lgkmcnt(8)\n"
  "v_mfma_f32_32x32x16_bf16 v[32:47], a[16:19], v[164:167], v[32:47]\n"
  "v_mfma_f32_32x32x16_bf16 v[16:31], a[20:23], v[164:167], v[16:31]\n"
  "s_waitcnt vmcnt(14)\n"
  "s_barrier\n"
  "ds_read_b64_tr_b16 a[8:9], v80 offset:24624\n"
  "ds_read_b64_tr_b16 a[10:11], v124 offset:24624\n"
  "ds_read_b64_tr_b16 a[12:13], v98 offset:25648\n"
  "ds_read_b64_tr_b16 a[14:15], v98 offset:26160\n"
  "s_waitcnt lgkmcnt(8)\n"
  "v_mfma_f32_32x32x16_bf16 v[32:47], a[24:27], v[168:171], v[32:47]\n"
  "ds_read_b128 v[176:179], v94 offset:0\n"
  "ds_read_b128 v[180:183], v95 offset:0\n"
  "ds_read_b128 v[184:187], v96 offset:0\n"
  "ds_read_b128 v[188:191], v97 offset:0\n"
  "v_mfma_f32_32x32x16_bf16 v[16:31], a[28:31], v[168:171], v[16:31]\n"
  "ds_read_b64_tr_b16 a[16:17], v81 offset:24624\n"
  "ds_read_b64_tr_b16 a[18:19], v125 offset:24624\n"
  "ds_read_b64_tr_b16 a[20:21], v99 offset:25648\n"
  "ds_read_b64_tr_b16 a[22:23], v99 offset:26160\n"
  "s_waitcnt lgkmcnt(12)\n"
  "v_mfma_f32_32x32x16_bf16 v[32:47], a[32:35], v[172:175], v[32:47]\n"
  "ds_read_b64_tr_b16 a[24:25], v82 offset:24624\n"
  "ds_read_b64_tr_b16 a[26:27], v126 offset:24624\n"
  "ds_read_b64_tr_b16 a[28:29], v100 offset:25648\n"
  "ds_read_b64_tr_b16 a[30:31], v100 offset:26160\n"
  "v_mfma_f32_32x32x16_bf16 v[16:31], a[36:39], v[172:175], v[16:31]\n"
  "s_waitcnt lgkmcnt(14)\n"
  "v_mfma_f32_32x32x16_bf16 a[48:63], v[84:87], a[8:11], a[48:63]\n"
  "ds_read_b64_tr_b16 a[32:33], v83 offset:24624\n"
  "ds_read_b64_tr_b16 a[34:35], v127 offset:24624\n"
  "ds_read_b64_tr_b16 a[36:37], v101 offset:25648\n"
  "ds_read_b64_tr_b16 a[38:39], v101 offset:26160\n"
  "s_mul_i32 s59, s77, 172032\n"
  "s_add_u32 m0, s87, 50272\n"
  "s_add_u32 s59, s59, s69\n"
  "buffer_load_dwordx4 v92, s[8:11], s59 offen offset:0 lds\n"
  "s_waitcnt lgkmcnt(15)\n"
  "v_mfma_f32_32x32x16_bf16 a[64:79], v[84:87], a[12:15], a[64:79]\n"
  "ds_read_u16 v64, v110 offset:0\n"
  "ds_read_u16 v65, v110 offset:56\n"
  "ds_read_u16 v66, v110 offset:112\n"
  "s_add_u32 m0, s87, 49248\n"
  "s_add_u32 s59, s59, 896\n"
  "buffer_load_dwordx4 v92, s[8:11], s59 offen offset:0 lds\n"
  "s_add_u32 s77, s77, 1\n"
  "s_cmp_eq_u32 s77, 7\n"
  "s_cselect_b32 s77, 0, s77\n"
  "v_mfma_f32_32x32x16_bf16 v[32:47], a[8:11], v[176:179], v[32:47]\n"
  "ds_read_u16 v67, v110 offset:168\n"
  "ds_read_u16 v68, v110 offset:448\n"
  "ds_read_u16 v69, v110 offset:504\n"
  "s_lshl_b32 s58, s56, 7\n"
  "s_add_u32 s58, s58, s68\n"
  "s_add_u32 s56, s56, 1\n"
  "s_cmp_eq_u32 s56, 7\n"
  "s_cselect_b32 s56, 0, s56\n"
  "s_add_u32 m0, s73, 12288\n"
  "s_nop 0\n"
  "buffer_load_dwordx4 v88, s[4:7], s58 offen offset:0 lds\n"
  "v_mfma_f32_32x32x16_bf16 v[16:31], a[12:15], v[176:179], v[16:31]\n"
  "ds_read_u16 v70, v110 offset:560\n"
  "ds_read_u16 v71, v110 offset:616\n"
  "ds_read_u16 v72, v110 offset:896\n"
  "buffer_load_dwordx4 v89, s[4:7], s58 offen offset:1024 lds\n"
  "v_mfma_f32_32x32x16_bf16 v[48:63], v[176:179], v[176:179], v[48:63]\n"
  "ds_read_u16 v73, v110 offset:952\n"
  "ds_read_u16 v74, v110 offset:1008\n"
  "ds_read_u16 v75, v110 offset:1064\n"
  "buffer_load_dwordx4 v90, s[4:7], s58 offen offset:2048 lds\n"
  "s_waitcnt lgkmcnt(15)\n"
  "v_mfma_f32_32x32x16_bf16 v[48:63], v[180:183], v[180:183], v[48:63]\n"
  "ds_read_u16 v76, v110 offset:1344\n"
  "ds_read_u16 v77, v110 offset:1400\n"
  "ds_read_u16 v78, v110 offset:1456\n"
  "buffer_load_dwordx4 v91, s[4:7], s58 offen offset:3072 lds\n"
  "v_mfma_f32_32x32x16_bf16 v[48:63], v[184:187], v[184:187], v[48:63]\n"
  "ds_read_u16 v79, v110 offset:1512\n"
  "v_mfma_f32_32x32x16_bf16 v[48:63], v[188:191], v[188:191], v[48:63]\n"
  "v_mfma_f32_32x32x16_bf16 v[32:47], a[16:19], v[180:183], v[32:47]\n"
  "v_mfma_f32_32x32x16_bf16 v[16:31], a[20:23], v[180:183], v[16:31]\n"
  "s_waitcnt vmcnt(14)\n"
  "s_barrier\n"
  "ds_read_b64_tr_b16 a[0:1], v80 offset:32832\n"
  "ds_read_b64_tr_b16 a[2:3], v124 offset:32832\n"
  "ds_read_b64_tr_b16 a[4:5], v98 offset:33856\n"
  "ds_read_b64_tr_b16 a[6:7], v98 offset:34368\n"
  "v_mfma_f32_32x32x16_bf16 v[32:47], a[24:27], v[184:187], v[32:47]\n"
  "ds_read_b128 v[192:195], v94 offset:4096\n"
  "ds_read_b128 v[196:199], v95 offset:4096\n"
  "ds_read_b128 v[200:203], v96 offset:4096\n"
  "ds_read_b128 v[204:207], v97 offset:4096\n"
  "v_mfma_f32_32x32x16_bf16 v[16:31], a[28:31], v[184:187], v[16:31]\n"
  "ds_read_b64_tr_b16 a[16:17], v81 offset:32832\n"
  "ds_read_b64_tr_b16 a[18:19], v125 offset:32832\n"
  "ds_read_b64_tr_b16 a[20:21], v99 offset:33856\n"
  "ds_read_b64_tr_b16 a[22:23], v99 offset:34368\n"
  "s_waitcnt lgkmcnt(15)\n"
  "v_mfma_f32_32x32x16_bf16 v[32:47], a[32:35], v[188:191], v[32:47]\n"
  "ds_read_b64_tr_b16 a[24:25], v82 offset:32832\n"
  "ds_read_b64_tr_b16 a[26:27], v126 offset:32832\n"
  "ds_read_b64_tr_b16 a[28:29], v100 offset:33856\n"
  "ds_read_b64_tr_b16 a[30:31], v100 offset:34368\n"
  "v_mfma_f32_32x32x16_bf16 v[16:31], a[36:39], v[188:191], v[16:31]\n"
  "s_waitcnt lgkmcnt(14)\n"
  "v_mfma_f32_32x32x16_bf16 a[48:63], v[84:87], a[0:3], a[48:63]\n"
  "ds_read_b64_tr_b16 a[32:33], v83 offset:32832\n"
  "ds_read_b64_tr_b16 a[34:35], v127 offset:32832\n"
  "ds_read_b64_tr_b16 a[36:37], v101 offset:33856\n"
  "ds_read_b64_tr_b16 a[38:39], v101 offset:34368\n"
  "s_sub_u32 s69, s69, 896\n"
  "s_mul_i32 s59, s77, 172032\n"
  "s_add_u32 m0, s73, 16384\n"
  "s_add_u32 s59, s59, s69\n"
  "buffer_load_dwordx4 v92, s[8:11], s59 offen offset:0 lds\n"
  "s_add_u32 s77, s77, 1\n"
  "s_cmp_eq_u32 s77, 7\n"
  "s_cselect_b32 s77, 0, s77\n"
  "s_waitcnt lgkmcnt(15)\n"
  "v_mfma_f32_32x32x16_bf16 a[64:79], v[84:87], a[4:7], a[64:79]\n"
  "v_lshl_or_b32 v116, v65, 16, v64\n"
  "v_lshl_or_b32 v117, v67, 16, v66\n"
  "v_mfma_f32_32x32x16_bf16 v[32:47], a[0:3], v[192:195], v[32:47]\n"
  "v_lshl_or_b32 v118, v69, 16, v68\n"
  "v_lshl_or_b32 v119, v71, 16, v70\n"
  "s_mul_i32 s59, s77, 172032\n"
  "s_add_u32 m0, s73, 17408\n"
  "s_add_u32 s59, s59, s69\n"
  "buffer_load_dwordx4 v92, s[8:11], s59 offen offset:0 lds\n"
  "s_add_u32 s77, s77, 1\n"
  "s_cmp_eq_u32 s77, 7\n"
  "s_cselect_b32 s77, 0, s77\n"
  "v_mfma_f32_32x32x16_bf16 v[16:31], a[4:7], v[192:195], v[16:31]\n"
  "v_lshl_or_b32 v120, v73, 16, v72\n"
  "v_lshl_or_b32 v121, v75, 16, v74\n"
  "v_mfma_f32_32x32x16_bf16 v[48:63], v[192:195], v[192:195], v[48:63]\n"
  "v_lshl_or_b32 v122, v77, 16, v76\n"
  "v_lshl_or_b32 v123, v79, 16, v78\n"
  "s_waitcnt lgkmcnt(14)\n"
  "v_mfma_f32_32x32x16_bf16 v[48:63], v[196:199], v[196:199], v[48:63]\n"
  "s_mul_i32 s59, s77, 172032\n"
  "s_add_u32 m0, s73, 18432\n"
  "s_add_u32 s59, s59, s69\n"
  "buffer_load_dwordx4 v92, s[8:11], s59 offen offset:0 lds\n"
  "s_add_u32 s77, s77, 1\n"
  "s_cmp_eq_u32 s77, 7\n"
  "s_cselect_b32 s77, 0, s77\n"
  "s_waitcnt lgkmcnt(13)\n"
  "v_mfma_f32_32x32x16_bf16 v[48:63], v[200:203], v[200:203], v[48:63]\n"
  "s_waitcnt lgkmcnt(12)\n"
  "v_mfma_f32_32x32x16_bf16 v[48:63], v[204:207], v[204:207], v[48:63]\n"
  "s_waitcnt lgkmcnt(8)\n"
  "v_mfma_f32_32x32x16_bf16 v[32:47], a[16:19], v[196:199], v[32:47]\n"
  "v_mfma_f32_32x32x16_bf16 v[16:31], a[20:23], v[196:199], v[16:31]\n"
  "s_waitcnt vmcnt(11)\n"
  "s_barrier\n"
  "ds_read_b64_tr_b16 a[8:9], v80 offset:41040\n"
  "ds_read_b64_tr_b16 a[10:11], v124 offset:41040\n"
  "ds_read_b64_tr_b16 a[12:13], v98 offset:42064\n"
  "ds_read_b64_tr_b16 a[14:15], v98 offset:42576\n"
  "s_waitcnt lgkmcnt(8)\n"
  "v_mfma_f32_32x32x16_bf16 v[32:47], a[24:27], v[200:203], v[32:47]\n"
  "ds_read_b128 v[208:211], v94 offset:8192\n"
  "ds_read_b128 v[212:215], v95 offset:8192\n"
  "ds_read_b128 v[216:219], v96 offset:8192\n"
  "ds_read_b128 v[220:223], v97 offset:8192\n"
  "v_mfma_f32_32x32x16_bf16 v[16:31], a[28:31], v[200:203], v[16:31]\n"
  "ds_read_b64_tr_b16 a[16:17], v81 offset:41040\n"
  "ds_read_b64_tr_b16 a[18:19], v125 offset:41040\n"
  "ds_read_b64_tr_b16 a[20:21], v99 offset:42064\n"
  "ds_read_b64_tr_b16 a[22:23], v99 offset:42576\n"
  "s_waitcnt lgkmcnt(12)\n"
  "v_mfma_f32_32x32x16_bf16 v[32:47], a[32:35], v[204:207], v[32:47]\n"
  "ds_read_b64_tr_b16 a[24:25], v82 offset:41040\n"
  "ds_read_b64_tr_b16 a[26:27], v126 offset:41040\n"
  "ds_read_b64_tr_b16 a[28:29], v100 offset:42064\n"
  "ds_read_b64_tr_b16 a[30:31], v100 offset:42576\n"
  "v_mfma_f32_32x32x16_bf16 v[16:31], a[36:39], v[204:207], v[16:31]\n"
  "s_waitcnt lgkmcnt(14)\n"
  "v_mfma_f32_32x32x16_bf16 a[48:63], v[84:87], a[8:11], a[48:63]\n"
  "ds_read_b64_tr_b16 a[32:33], v83 offset:41040\n"
  "ds_read_b64_tr_b16 a[34:35], v127 offset:41040\n"
  "ds_read_b64_tr_b16 a[36:37], v101 offset:42064\n"
  "ds_read_b64_tr_b16 a[38:39], v101 offset:42576\n"
  "s_mul_i32 s59, s77, 172032\n"
  "s_add_u32 m0, s73, 19456\n"
  "s_add_u32 s59, s59, s69\n"
  "buffer_load_dwordx4 v92, s[8:11], s59 offen offset:0 lds\n"
  "s_add_u32 s77, s77, 1\n"
  "s_cmp_eq_u32 s77, 7\n"
  "s_cselect_b32 s77, 0, s77\n"
  "s_waitcnt lgkmcnt(15)\n"
  "v_mfma_f32_32x32x16_bf16 a[64:79], v[84:87], a[12:15], a[64:79]\n"
  "v_and_b32 v6, 31, v1\n"
  "v_lshrrev_b32 v7, 5, v1\n"
  "v_lshlrev_b32 v6, 4, v6\n"
  "v_mfma_f32_32x32x16_bf16 v[32:47], a[8:11], v[208:211], v[32:47]\n"
  "s_lshl_b32 s60, s52, 2\n"
  "s_add_u32 s60, s60, 160368\n"
  "v_lshl_add_u32 v7, v7, 9, v6\n"
  "v_mfma_f32_32x32x16_bf16 v[16:31], a[12:15], v[208:211], v[16:31]\n"
  "v_add_u32 v114, s60, v7\n"
  "v_add_u32 v115, 160368, v6\n"
  "v_mfma_f32_32x32x16_bf16 v[48:63], v[208:211], v[208:211], v[48:63]\n"
  "s_mov_b32 s64, 0xf0f0f0f0\n"
  "s_mov_b32 s65, 0xf0f0f0f0\n"
  "s_mov_b32 s66, 0xffff0000\n"
  "s_mov_b32 s67, 0xffff0000\n"
  "s_mul_i32 s59, s77, 172032\n"
  "s_add_u32 m0, s73, 20480\n"
  "s_add_u32 s59, s59, s69\n"
  "buffer_load_dwordx4 v92, s[8:11], s59 offen offset:0 lds\n"
  "s_add_u32 s77, s77, 1\n"
  "s_cmp_eq_u32 s77, 7\n"
  "s_cselect_b32 s77, 0, s77\n"
  "s_waitcnt lgkmcnt(14)\n"
  "v_mfma_f32_32x32x16_bf16 v[48:63], v[212:215], v[212:215], v[48:63]\n"
  "s_waitcnt lgkmcnt(13)\n"
  "v_mfma_f32_32x32x16_bf16 v[48:63], v[216:219], v[216:219], v[48:63]\n"
  "s_waitcnt lgkmcnt(12)\n"
  "v_mfma_f32_32x32x16_bf16 v[48:63], v[220:223], v[220:223], v[48:63]\n"
  "s_waitcnt lgkmcnt(8)\n"
  "v_mfma_f32_32x32x16_bf16 v[32:47], a[16:19], v[212:215], v[32:47]\n"
  "v_mfma_f32_32x32x16_bf16 v[16:31], a[20:23], v[212:215], v[16:31]\n"
  "v_mov_b32 v106, s44\n"
  "v_mov_b32 v107, s45\n"
  "v_cmp_eq_u32 vcc, 0, %2\n"
  "v_mov_b32 v5, 2147483632\n"
  "s_lshl_b32 s58, %1, 3\n"
  "v_cndmask_b32 v5, v5, 0, vcc\n"
  "s_add_u32 s58, s58, 4096\n"
  "s_waitcnt vmcnt(5)\n"
  "s_barrier\n"
  "buffer_store_dwordx2 v[106:107], v5, s[12:15], s58 offen sc1\n"
  "ds_read_b64_tr_b16 a[0:1], v80 offset:49248\n"
  "ds_read_b64_tr_b16 a[2:3], v124 offset:49248\n"
  "ds_read_b64_tr_b16 a[4:5], v98 offset:50272\n"
  "ds_read_b64_tr_b16 a[6:7], v98 offset:50784\n"
  "s_waitcnt lgkmcnt(8)\n"
  "v_mfma_f32_32x32x16_bf16 v[32:47], a[24:27], v[216:219], v[32:47]\n"
  "ds_read_b128 v[224:227], v94 offset:12288\n"
  "ds_read_b128 v[228:231], v95 offset:12288\n"
  "ds_read_b128 v[232:235], v96 offset:12288\n"
  "ds_read_b128 v[236:239], v97 offset:12288\n"
  "v_mfma_f32_32x32x16_bf16 v[16:31], a[28:31], v[216:219], v[16:31]\n"
  "ds_read_b64_tr_b16 a[16:17], v81 offset:49248\n"
  "ds_read_b64_tr_b16 a[18:19], v125 offset:49248\n"
  "ds_read_b64_tr_b16 a[20:21], v99 offset:50272\n"
  "ds_read_b64_tr_b16 a[22:23], v99 offset:50784\n"
  "s_waitcnt lgkmcnt(12)\n"
  "v_mfma_f32_32x32x16_bf16 v[32:47], a[32:35], v[220:223], v[32:47]\n"
  "ds_read_b64_tr_b16 a[24:25], v82 offset:49248\n"
  "ds_read_b64_tr_b16 a[26:27], v126 offset:49248\n"
  "ds_read_b64_tr_b16 a[28:29], v100 offset:50272\n"
  "ds_read_b64_tr_b16 a[30:31], v100 offset:50784\n"
  "v_mfma_f32_32x32x16_bf16 v[16:31], a[36:39], v[220:223], v[16:31]\n"
  "s_waitcnt lgkmcnt(14)\n"
  "v_mfma_f32_32x32x16_bf16 a[48:63], v[84:87], a[0:3], a[48:63]\n"
  "ds_read_b64_tr_b16 a[32:33], v83 offset:49248\n"
  "ds_read_b64_tr_b16 a[34:35], v127 offset:49248\n"
  "ds_read_b64_tr_b16 a[36:37], v101 offset:50272\n"
  "ds_read_b64_tr_b16 a[38:39], v101 offset:50784\n"
  "s_mul_i32 s59, s77, 172032\n"
  "s_add_u32 m0, s73, 21504\n"
  "s_add_u32 s59, s59, s69\n"
  "buffer_load_dwordx4 v92, s[8:11], s59 offen offset:0 lds\n"
  "s_add_u32 s77, s77, 1\n"
  "s_cmp_eq_u32 s77, 7\n"
  "s_cselect_b32 s77, 0, s77\n"
  "s_waitcnt lgkmcnt(15)\n"
  "v_mfma_f32_32x32x16_bf16 a[64:79], v[84:87], a[4:7], a[64:79]\n"
  "v_mfma_f32_32x32x16_bf16 v[32:47], a[0:3], v[224:227], v[32:47]\n"
  "v_mfma_f32_32x32x16_bf16 v[16:31], a[4:7], v[224:227], v[16:31]\n"
  "s_nop 9\n"
  "v_accvgpr_read_b32 v112, a64\n"
  "v_accvgpr_read_b32 v113, a48\n"
  "v_mov_b32 v102, v115\n"
  "s_mul_i32 s59, s77, 172032\n"
  "s_add_u32 m0, s73, 22528\n"
  "s_add_u32 s59, s59, s69\n"
  "buffer_load_dwordx4 v92, s[8:11], s59 offen offset:0 lds\n"
  "s_add_u32 s77, s77, 1\n"
  "s_cmp_eq_u32 s77, 7\n"
  "s_cselect_b32 s77, 0, s77\n"
  "v_mfma_f32_32x32x16_bf16 v[48:63], v[224:227], v[224:227], v[48:63]\n"
  "v_cndmask_b32 v112, v112, v113, s[74:75]\n"
  "v_cvt_pk_bf16_f32 v5, v112, v112\n"
  "v_and_b32 v5, 0xffff0000, v5\n"
  "v_sub_f32 v5, v112, v5\n"
  "v_cvt_pk_bf16_f32 v5, v112, v5\n"
  "ds_write_b32 v114, v5\n"
  "s_waitcnt lgkmcnt(15)\n"
  "v_mfma_f32_32x32x16_bf16 v[48:63], v[228:231], v[228:231], v[48:63]\n"
  "s_waitcnt lgkmcnt(14)\n"
  "v_mfma_f32_32x32x16_bf16 v[48:63], v[232:235], v[232:235], v[48:63]\n"
  "s_waitcnt lgkmcnt(0)\n"
  "s_barrier\n"
  "v_mfma_f32_32x32x16_bf16 v[48:63], v[236:239], v[236:239], v[48:63]\n"
  "ds_read_b128 v[240:243], v102\n"
  "ds_read_b128 v[244:247], v102 offset:512\n"
  "v_mfma_f32_32x32x16_bf16 v[32:47], a[16:19], v[228:231], v[32:47]\n"
  "s_mov_b32 s58, 0xaaaaaaaa\n"
  "s_mov_b32 s59, 0xaaaaaaaa\n"
  "s_mov_b32 s60, 0xcccccccc\n"
  "s_mov_b32 s61, 0xcccccccc\n"
  "s_mov_b32 s62, 0xff00ff00\n"
  "s_mov_b32 s63, 0xff00ff00\n"
  "v_mfma_f32_32x32x16_bf16 v[32:47], a[24:27], v[232:235], v[32:47]\n"
  "s_nop 1\n"
  "v_cndmask_b32 v48, v48, v49, s[58:59]\n"
  "v_cndmask_b32 v49, v50, v51, s[58:59]\n"
  "v_cndmask_b32 v50, v52, v53, s[58:59]\n"
  "v_cndmask_b32 v51, v54, v55, s[58:59]\n"
  "v_cndmask_b32 v52, v56, v57, s[58:59]\n"
  "v_mfma_f32_32x32x16_bf16 v[32:47], a[32:35], v[236:239], v[32:47]\n"
  "v_cndmask_b32 v53, v58, v59, s[58:59]\n"
  "v_cndmask_b32 v54, v60, v61, s[58:59]\n"
  "v_cndmask_b32 v55, v62, v63, s[58:59]\n"
  "v_cndmask_b32 v48, v48, v49, s[60:61]\n"
  "v_cndmask_b32 v49, v50, v51, s[60:61]\n"
  "v_mfma_f32_32x32x16_bf16 v[16:31], a[20:23], v[228:231], v[16:31]\n"
  "v_cndmask_b32 v50, v52, v53, s[60:61]\n"
  "v_cndmask_b32 v51, v54, v55, s[60:61]\n"
  "v_cndmask_b32 v48, v48, v49, s[62:63]\n"
  "v_cndmask_b32 v49, v50, v51, s[62:63]\n"
  "v_cndmask_b32 v48, v48, v49, s[66:67]\n"
  "v_mfma_f32_32x32x16_bf16 v[16:31], a[28:31], v[232:235], v[16:31]\n"
  "v_mov_b32 v49, v48\n"
  "v_mov_b32 v5, v44\n"
  "v_mov_b32 v6, v44\n"
  "v_permlane32_swap_b32 v48, v49\n"
  "s_nop 0\n"
  "v_permlane32_swap_b32 v5, v6\n"
  "v_mfma_f32_32x32x16_bf16 v[16:31], a[36:39], v[236:239], v[16:31]\n"
  "v_cndmask_b32 v48, v48, v49, s[64:65]\n"
  "v_mul_f32 v6, -2.2321428571e-03, v6\n"
  "v_mul_f32 v48, 2.2321428571e-03, v48\n"
  "v_cvt_pk_bf16_f32 v5, v6, v6\n"
  "v_fma_f32 v48, -v6, v6, v48\n"
  "v_and_b32 v7, 0xffff0000, v5\n"
  "v_max_f32 v48, 0, v48\n"
  "v_sub_f32 v7, v6, v7\n"
  "v_add_f32 v48, 0x3727c5ac, v48\n"
  "v_cvt_pk_bf16_f32 v7, v7, v7\n"
  "v_rsq_f32 v81, v48\n"
  "v_cndmask_b32 v124, v5, v7, s[74:75]\n"
  "v_cndmask_b32 v125, v5, v7, s[74:75]\n"
  "v_cndmask_b32 v126, v5, v7, s[74:75]\n"
  "v_cndmask_b32 v127, v5, v7, s[74:75]\n"
  "s_waitcnt lgkmcnt(0)\n"
  "s_nop 0\n"
  "v_mfma_f32_32x32x16_bf16 v[16:31], v[240:243], v[124:127], v[16:31]\n"
  "v_mfma_f32_32x32x16_bf16 v[32:47], v[244:247], v[124:127], v[32:47]\n"
  "v_mul_f32 v80, 0x3fb8aa3b, v81\n"
  "v_and_b32 v5, 31, v1\n"
  "v_mul_u32_u24 v5, 200, v5\n"
  "v_lshrrev_b32 v6, 5, v1\n"
  "v_lshl_add_u32 v5, v6, 3, v5\n"
  "s_add_u32 s58, s73, 9984\n"
  "v_add_u32 v88, s58, v5\n"
  "s_nop 3\n"
  "v_cvt_pk_bf16_f32 v48, v16, v17\n"
  "v_cvt_pk_bf16_f32 v49, v18, v19\n"
  "v_cvt_pk_bf16_f32 v50, v20, v21\n"
  "v_cvt_pk_bf16_f32 v51, v22, v23\n"
  "v_cvt_pk_bf16_f32 v52, v24, v25\n"
  "v_cvt_pk_bf16_f32 v53, v26, v27\n"
  "v_cvt_pk_bf16_f32 v54, v28, v29\n"
  "v_cvt_pk_bf16_f32 v55, v30, v31\n"
  "v_cndmask_b32 v54, v54, 0, s[74:75]\n"
  "v_cndmask_b32 v55, v55, 0, s[74:75]\n"
  "v_mfma_f32_32x32x16_bf16 v[56:71], v[116:119], v[48:51], 0\n"
  "s_nop 0\n"
  "v_mfma_f32_32x32x16_bf16 v[56:71], v[120:123], v[52:55], v[56:71]\n"
  "v_pk_mul_f32 v[32:33], v[32:33], v[80:81] op_sel:[0,1] op_sel_hi:[1,1]\n"
  "v_pk_mul_f32 v[34:35], v[34:35], v[80:81] op_sel:[0,1] op_sel_hi:[1,1]\n"
  "v_pk_mul_f32 v[36:37], v[36:37], v[80:81] op_sel:[0,1] op_sel_hi:[1,1]\n"
  "v_pk_mul_f32 v[38:39], v[38:39], v[80:81] op_sel:[0,1] op_sel_hi:[1,1]\n"
  "v_pk_mul_f32 v[40:41], v[40:41], v[80:81] op_sel:[0,1] op_sel_hi:[1,1]\n"
  "v_pk_mul_f32 v[42:43], v[42:43], v[80:81] op_sel:[0,1] op_sel_hi:[1,1]\n"
  "v_pk_mul_f32 v[44:45], v[44:45], v[80:81] op_sel:[0,1] op_sel_hi:[1,1]\n"
  "v_pk_mul_f32 v[46:47], v[46:47], v[80:81] op_sel:[0,1] op_sel_hi:[1,1]\n"
  "v_cvt_pk_bf16_f32 v72, v32, v33\n"
  "v_cvt_pk_bf16_f32 v73, v34, v35\n"
  "v_cvt_pk_bf16_f32 v74, v36, v37\n"
  "v_cvt_pk_bf16_f32 v75, v38, v39\n"
  "v_cvt_pk_bf16_f32 v76, v40, v41\n"
  "v_cvt_pk_bf16_f32 v77, v42, v43\n"
  "v_cvt_pk_bf16_f32 v78, v44, v45\n"
  "v_cvt_pk_bf16_f32 v79, v46, v47\n"
  "v_cndmask_b32 v78, v78, v104, s[74:75]\n"
  "ds_write_b64 v88, v[72:73] offset:128\n"
  "ds_write_b64 v88, v[74:75] offset:144\n"
  "ds_write_b64 v88, v[76:77] offset:160\n"
  "ds_write_b64 v88, v[78:79] offset:176\n"
  "v_max3_f32 v106, v56, v57, v58\n"
  "v_max3_f32 v107, -v56, -v57, -v58\n"
  "v_max3_f32 v106, v106, v59, v60\n"
  "v_max3_f32 v107, v107, -v59, -v60\n"
  "v_max3_f32 v106, v106, v61, v62\n"
  "v_max3_f32 v107, v107, -v61, -v62\n"
  "v_max3_f32 v106, v106, v64, v65\n"
  "v_max3_f32 v107, v107, -v64, -v65\n"
  "v_max3_f32 v106, v106, v66, v67\n"
  "v_max3_f32 v107, v107, -v66, -v67\n"
  "v_max3_f32 v106, v106, v68, v69\n"
  "v_max3_f32 v107, v107, -v68, -v69\n"
  "v_max3_f32 v106, v106, v70, v70\n"
  "v_max3_f32 v107, v107, -v70, -v70\n"
  "s_nop 1\n"
  "v_permlane32_swap_b32 v106, v107\n"
  "v_max_f32 v106, v106, v107\n"
  "v_mul_f32 v82, -v106, v80\n"
  "v_mov_b32 v83, v82\n"
  "s_nop 1\n"
  "v_permlane32_swap_b32 v82, v83\n"
  "v_pk_fma_f32 v[16:17], v[56:57], v[80:81], v[82:83] op_sel_hi:[1,0,0]\n"
  "v_pk_fma_f32 v[56:57], v[56:57], v[80:81], v[82:83] op_sel:[0,0,1] op_sel_hi:[1,0,1] neg_lo:[1,0,0] neg_hi:[1,0,0]\n"
  "v_pk_fma_f32 v[18:19], v[58:59], v[80:81], v[82:83] op_sel_hi:[1,0,0]\n"
  "v_pk_fma_f32 v[58:59], v[58:59], v[80:81], v[82:83] op_sel:[0,0,1] op_sel_hi:[1,0,1] neg_lo:[1,0,0] neg_hi:[1,0,0]\n"
  "v_pk_fma_f32 v[20:21], v[60:61], v[80:81], v[82:83] op_sel_hi:[1,0,0]\n"
  "v_pk_fma_f32 v[60:61], v[60:61], v[80:81], v[82:83] op_sel:[0,0,1] op_sel_hi:[1,0,1] neg_lo:[1,0,0] neg_hi:[1,0,0]\n"
  "v_pk_fma_f32 v[22:23], v[62:63], v[80:81], v[82:83] op_sel_hi:[1,0,0]\n"
  "v_pk_fma_f32 v[62:63], v[62:63], v[80:81], v[82:83] op_sel:[0,0,1] op_sel_hi:[1,0,1] neg_lo:[1,0,0] neg_hi:[1,0,0]\n"
  "v_pk_fma_f32 v[24:25], v[64:65], v[80:81], v[82:83] op_sel_hi:[1,0,0]\n"
  "v_pk_fma_f32 v[64:65], v[64:65], v[80:81], v[82:83] op_sel:[0,0,1] op_sel_hi:[1,0,1] neg_lo:[1,0,0] neg_hi:[1,0,0]\n"
  "v_pk_fma_f32 v[26:27], v[66:67], v[80:81], v[82:83] op_sel_hi:[1,0,0]\n"
  "v_pk_fma_f32 v[66:67], v[66:67], v[80:81], v[82:83] op_sel:[0,0,1] op_sel_hi:[1,0,1] neg_lo:[1,0,0] neg_hi:[1,0,0]\n"
  "v_pk_fma_f32 v[28:29], v[68:69], v[80:81], v[82:83] op_sel_hi:[1,0,0]\n"
  "v_pk_fma_f32 v[68:69], v[68:69], v[80:81], v[82:83] op_sel:[0,0,1] op_sel_hi:[1,0,1] neg_lo:[1,0,0] neg_hi:[1,0,0]\n"
  "v_pk_fma_f32 v[30:31], v[70:71], v[80:81], v[82:83] op_sel_hi:[1,0,0]\n"
  "v_pk_fma_f32 v[70:71], v[70:71], v[80:81], v[82:83] op_sel:[0,0,1] op_sel_hi:[1,0,1] neg_lo:[1,0,0] neg_hi:[1,0,0]\n"
  "v_exp_f32 v16, v16\n"
  "s_mul_i32 s62, s97, 14336\n"
  "s_mul_i32 s63, s98, 1792\n"
  "s_add_u32 s62, s62, s63\n"
  "buffer_load_dword v15, v103, s[28:31], s62 offen\n"
  "v_exp_f32 v56, v56\n"
  "v_exp_f32 v17, v17\n"
  "v_exp_f32 v57, v57\n"
  "v_exp_f32 v18, v18\n"
  "v_exp_f32 v58, v58\n"
  "v_exp_f32 v19, v19\n"
  "v_exp_f32 v59, v59\n"
  "v_exp_f32 v20, v20\n"
  "v_exp_f32 v60, v60\n"
  "v_exp_f32 v21, v21\n"
  "v_exp_f32 v61, v61\n"
  "v_exp_f32 v22, v22\n"
  "v_exp_f32 v62, v62\n"
  "v_mov_b32 v23, 0\n"
  "v_mov_b32 v63, 0\n"
  "v_exp_f32 v24, v24\n"
  "v_exp_f32 v64, v64\n"
  "v_exp_f32 v25, v25\n"
  "v_exp_f32 v65, v65\n"
  "v_exp_f32 v26, v26\n"
  "v_exp_f32 v66, v66\n"
  "v_exp_f32 v27, v27\n"
  "v_exp_f32 v67, v67\n"
  "v_exp_f32 v28, v28\n"
  "v_exp_f32 v68, v68\n"
  "v_exp_f32 v29, v29\n"
  "v_exp_f32 v69, v69\n"
  "v_exp_f32 v30, v30\n"
  "v_exp_f32 v70, v70\n"
  "v_mov_b32 v31, 0\n"
  "v_mov_b32 v71, 0\n"
  "v_pk_add_f32 v[82:83], v[16:17], v[18:19]\n"
  "v_pk_add_f32 v[84:85], v[56:57], v[58:59]\n"
  "v_pk_add_f32 v[82:83], v[82:83], v[20:21]\n"
  "v_pk_add_f32 v[84:85], v[84:85], v[60:61]\n"
  "v_pk_add_f32 v[82:83], v[82:83], v[22:23]\n"
  "buffer_store_dwordx4 a[80:83], v2, s[12:15], s80 offen sc1\n"
  "v_pk_add_f32 v[84:85], v[84:85], v[62:63]\n"
  "v_pk_add_f32 v[82:83], v[82:83], v[24:25]\n"
  "v_pk_add_f32 v[84:85], v[84:85], v[64:65]\n"
  "v_pk_add_f32 v[82:83], v[82:83], v[26:27]\n"
  "v_pk_add_f32 v[84:85], v[84:85], v[66:67]\n"
  "v_pk_add_f32 v[82:83], v[82:83], v[28:29]\n"
  "v_pk_add_f32 v[84:85], v[84:85], v[68:69]\n"
  "v_pk_add_f32 v[82:83], v[82:83], v[30:31]\n"
  "v_pk_add_f32 v[84:85], v[84:85], v[70:71]\n"
  "v_add_f32 v106, v82, v83\n"
  "v_add_f32 v107, v84, v85\n"
  "s_nop 1\n"
  "v_permlane32_swap_b32 v106, v107\n"
  "v_add_f32 v106, v106, v107\n"
  "v_rcp_f32 v86, v106\n"
  "s_nop 0\n"
  "v_mov_b32 v87, v86\n"
  "s_nop 1\n"
  "v_permlane32_swap_b32 v86, v87\n"
  "v_pk_mul_f32 v[16:17], v[16:17], v[86:87] op_sel_hi:[1,0]\n"
  "v_pk_mul_f32 v[56:57], v[56:57], v[86:87] op_sel:[0,1] op_sel_hi:[1,1]\n"
  "v_pk_mul_f32 v[18:19], v[18:19], v[86:87] op_sel_hi:[1,0]\n"
  "v_pk_mul_f32 v[58:59], v[58:59], v[86:87] op_sel:[0,1] op_sel_hi:[1,1]\n"
  "v_pk_mul_f32 v[20:21], v[20:21], v[86:87] op_sel_hi:[1,0]\n"
  "v_pk_mul_f32 v[60:61], v[60:61], v[86:87] op_sel:[0,1] op_sel_hi:[1,1]\n"
  "v_pk_mul_f32 v[22:23], v[22:23], v[86:87] op_sel_hi:[1,0]\n"
  "v_pk_mul_f32 v[62:63], v[62:63], v[86:87] op_sel:[0,1] op_sel_hi:[1,1]\n"
  "v_pk_mul_f32 v[24:25], v[24:25], v[86:87] op_sel_hi:[1,0]\n"
  "v_pk_mul_f32 v[64:65], v[64:65], v[86:87] op_sel:[0,1] op_sel_hi:[1,1]\n"
  "v_pk_mul_f32 v[26:27], v[26:27], v[86:87] op_sel_hi:[1,0]\n"
  "v_pk_mul_f32 v[66:67], v[66:67], v[86:87] op_sel:[0,1] op_sel_hi:[1,1]\n"
  "v_pk_mul_f32 v[28:29], v[28:29], v[86:87] op_sel_hi:[1,0]\n"
  "v_pk_mul_f32 v[68:69], v[68:69], v[86:87] op_sel:[0,1] op_sel_hi:[1,1]\n"
  "v_pk_mul_f32 v[30:31], v[30:31], v[86:87] op_sel_hi:[1,0]\n"
  "v_pk_mul_f32 v[70:71], v[70:71], v[86:87] op_sel:[0,1] op_sel_hi:[1,1]\n"
  "v_cvt_pk_bf16_f32 v240, v16, v17\n"
  "v_cvt_pk_bf16_f32 v248, v56, v57\n"
  "v_cvt_pk_bf16_f32 v241, v18, v19\n"
  "v_cvt_pk_bf16_f32 v249, v58, v59\n"
  "s_add_u32 s62, s80, 28672\n"
  "buffer_store_dwordx4 a[80:83], v2, s[12:15], s62 offen sc1\n"
  "v_cvt_pk_bf16_f32 v242, v20, v21\n"
  "v_cvt_pk_bf16_f32 v250, v60, v61\n"
  "v_cvt_pk_bf16_f32 v243, v22, v23\n"
  "v_cvt_pk_bf16_f32 v251, v62, v63\n"
  "v_cvt_pk_bf16_f32 v244, v24, v25\n"
  "v_cvt_pk_bf16_f32 v252, v64, v65\n"
  "v_cvt_pk_bf16_f32 v245, v26, v27\n"
  "v_cvt_pk_bf16_f32 v253, v66, v67\n"
  "v_cvt_pk_bf16_f32 v246, v28, v29\n"
  "v_cvt_pk_bf16_f32 v254, v68, v69\n"
  "v_cvt_pk_bf16_f32 v247, v30, v31\n"
  "v_cvt_pk_bf16_f32 v255, v70, v71\n"
  "ds_write_b64 v88, v[240:241] offset:0\n"
  "ds_write_b64 v88, v[242:243] offset:16\n"
  "ds_write_b64 v88, v[244:245] offset:32\n"
  "ds_write_b64 v88, v[246:247] offset:48\n"
  "ds_write_b64 v88, v[248:249] offset:64\n"
  "ds_write_b64 v88, v[250:251] offset:80\n"
  "ds_write_b64 v88, v[252:253] offset:96\n"
  "ds_write_b64 v88, v[254:255] offset:112\n"
  "v_lshrrev_b32 v5, 5, v1\n"
  "v_bfe_u32 v6, v1, 4, 1\n"
  "v_lshl_or_b32 v5, v6, 4, v5\n"
  "v_bfe_u32 v6, v1, 2, 2\n"
  "v_lshl_add_u32 v5, v6, 2, v5\n"
  "v_mul_u32_u24 v5, 200, v5\n"
  "v_add_u32 v5, 9984, v5\n"
  "v_and_b32 v6, 3, v1\n"
  "v_lshl_add_u32 v90, v6, 3, v5\n"
  "v_and_b32 v6, 1, v1\n"
  "v_lshl_add_u32 v5, v6, 4, v5\n"
  "v_bfe_u32 v6, v1, 1, 1\n"
  "v_lshl_add_u32 v5, v6, 3, v5\n"
  "s_lshl_b32 s58, s52, 5\n"
  "v_add_u32 v89, s58, v5\n"
  "v_add_u32 v91, 50432, v89\n"
  "v_add_u32 v92, 50432, v90\n"
  "s_waitcnt lgkmcnt(0)\n"
  "s_barrier\n"
  "ds_read_b64_tr_b16 v[16:17], v89 offset:0\n"
  "ds_read_b64_tr_b16 v[18:19], v89 offset:400\n"
  "ds_read_b64_tr_b16 v[32:33], v90 offset:128\n"
  "ds_read_b64_tr_b16 v[34:35], v90 offset:528\n"
  "ds_read_b64_tr_b16 v[36:37], v90 offset:160\n"
  "ds_read_b64_tr_b16 v[38:39], v90 offset:560\n"
  "ds_read_b64_tr_b16 v[20:21], v89 offset:25216\n"
  "ds_read_b64_tr_b16 v[22:23], v89 offset:25616\n"
  "ds_read_b64_tr_b16 v[40:41], v90 offset:25344\n"
  "ds_read_b64_tr_b16 v[42:43], v90 offset:25744\n"
  "ds_read_b64_tr_b16 v[44:45], v90 offset:25376\n"
  "ds_read_b64_tr_b16 v[46:47], v90 offset:25776\n"
  "ds_read_b64_tr_b16 v[24:25], v91 offset:0\n"
  "ds_read_b64_tr_b16 v[26:27], v91 offset:400\n"
  "ds_read_b64_tr_b16 v[48:49], v92 offset:128\n"
  "ds_read_b64_tr_b16 v[50:51], v92 offset:528\n"
  "ds_read_b64_tr_b16 v[52:53], v92 offset:160\n"
  "ds_read_b64_tr_b16 v[54:55], v92 offset:560\n"
  "ds_read_b64_tr_b16 v[28:29], v91 offset:25216\n"
  "ds_read_b64_tr_b16 v[30:31], v91 offset:25616\n"
  "ds_read_b64_tr_b16 v[56:57], v92 offset:25344\n"
  "ds_read_b64_tr_b16 v[58:59], v92 offset:25744\n"
  "ds_read_b64_tr_b16 v[60:61], v92 offset:25376\n"
  "ds_read_b64_tr_b16 v[62:63], v92 offset:25776\n"
  "s_and_b32 s61, s54, 1\n"
  "s_lshl_b32 s61, s61, 3\n"
  "s_add_u32 s61, s61, 16384\n"
  "s_add_u32 s58, s52, 0\n"
  "s_and_b32 s58, s58, 3\n"
  "s_mul_i32 s59, s58, 25216\n"
  "s_add_u32 s59, s59, s61\n"
  "v_add_u32 v98, s59, v3\n"
  "s_add_u32 s58, s52, 1\n"
  "s_and_b32 s58, s58, 3\n"
  "s_mul_i32 s59, s58, 25216\n"
  "s_add_u32 s59, s59, s61\n"
  "v_add_u32 v99, s59, v3\n"
  "s_add_u32 s58, s52, 2\n"
  "s_and_b32 s58, s58, 3\n"
  "s_mul_i32 s59, s58, 25216\n"
  "s_add_u32 s59, s59, s61\n"
  "v_add_u32 v100, s59, v3\n"
  "s_add_u32 s58, s52, 3\n"
  "s_and_b32 s58, s58, 3\n"
  "s_mul_i32 s59, s58, 25216\n"
  "s_add_u32 s59, s59, s61\n"
  "v_add_u32 v101, s59, v3\n"
  "s_waitcnt lgkmcnt(15)\n"
  "v_mfma_f32_16x16x32_bf16 v[64:67], v[16:19], v[32:35], 0\n"
  "v_mfma_f32_16x16x32_bf16 v[68:71], v[16:19], v[36:39], 0\n"
  "s_waitcnt lgkmcnt(14)\n"
  "v_mfma_f32_16x16x32_bf16 v[64:67], v[20:23], v[40:43], v[64:67]\n"
  "s_waitcnt lgkmcnt(12)\n"
  "v_mfma_f32_16x16x32_bf16 v[68:71], v[20:23], v[44:47], v[68:71]\n"
  "s_waitcnt lgkmcnt(8)\n"
  "v_mfma_f32_16x16x32_bf16 v[64:67], v[24:27], v[48:51], v[64:67]\n"
  "s_waitcnt lgkmcnt(6)\n"
  "v_mfma_f32_16x16x32_bf16 v[68:71], v[24:27], v[52:55], v[68:71]\n"
  "s_waitcnt lgkmcnt(2)\n"
  "v_mfma_f32_16x16x32_bf16 v[64:67], v[28:31], v[56:59], v[64:67]\n"
  "s_waitcnt lgkmcnt(0)\n"
  "v_mfma_f32_16x16x32_bf16 v[68:71], v[28:31], v[60:63], v[68:71]\n"
  "s_nop 5\n"
  "v_cvt_pk_bf16_f32 v76, v64, v65\n"
  "v_cvt_pk_bf16_f32 v77, v66, v67\n"
  "v_cvt_pk_bf16_f32 v78, v68, v69\n"
  "v_cvt_pk_bf16_f32 v79, v70, v71\n"
  "s_nop 0\n"
  "v_permlane16_swap_b32 v76, v78\n"
  "v_permlane16_swap_b32 v77, v79\n"
  "v_mov_b32_sdwa v79, s100 dst_sel:WORD_1 dst_unused:UNUSED_PRESERVE src0_sel:WORD_1\n"
  "buffer_store_dwordx4 v[76:79], v2, s[12:15], s78 offen offset:0 sc1\n"
  "v_lshlrev_b32 v94, 5, v1\n"
  "s_lshl_b32 s58, s84, 2\n"
  "s_add_u32 s58, s58, s52\n"
  "s_lshl_b32 s58, s58, 14\n"
  "s_add_u32 s58, s58, 1515520\n"
  "s_add_u32 s91, s58, s3\n"
  "s_lshl_b32 s58, s84, 2\n"
  "s_add_u32 s58, s58, s52\n"
  "s_mul_i32 s58, s58, 57344\n"
  "s_add_u32 s58, s58, 1974272\n"
  "s_add_u32 s96, s58, s3\n"
  "s_waitcnt vmcnt(4)\n"
  "ds_read_b64_tr_b16 v[48:49], v98 offset:0\n"
  "ds_read_b64_tr_b16 v[50:51], v98 offset:512\n"
  "ds_read_b64_tr_b16 v[52:53], v98 offset:1024\n"
  "ds_read_b64_tr_b16 v[54:55], v98 offset:1536\n"
  "ds_read_b64_tr_b16 v[56:57], v98 offset:2048\n"
  "ds_read_b64_tr_b16 v[58:59], v98 offset:2560\n"
  "ds_read_b64_tr_b16 v[60:61], v98 offset:3072\n"
  "ds_read_b64_tr_b16 v[62:63], v98 offset:3584\n"
  "ds_read_b64_tr_b16 v[64:65], v98 offset:4096\n"
  "ds_read_b64_tr_b16 v[66:67], v98 offset:4608\n"
  "ds_read_b64_tr_b16 v[68:69], v98 offset:5120\n"
  "ds_read_b64_tr_b16 v[70:71], v98 offset:5632\n"
  "ds_read_b64_tr_b16 v[72:73], v98 offset:6144\n"
  "ds_read_b64_tr_b16 v[74:75], v98 offset:6656\n"
  "buffer_store_dwordx2 a[80:81], v14, s[12:15], s81 offen sc1\n"
  "s_mul_i32 s89, s90, 229376\n"
  "s_add_u32 s89, s89, s88\n"
  "s_add_u32 s90, s90, 1\n"
  "s_cmp_eq_u32 s90, 7\n"
  "s_cselect_b32 s90, 0, s90\n"
  "s_add_u32 m0, s73, 0\n"
  "s_nop 0\n"
  "buffer_load_dwordx4 v8, s[28:31], s89 offen offset:0 lds\n"
  "v_mov_b32 v112, 0\n"
  "v_mov_b32 v113, 0\n"
  "v_mov_b32 v114, 0\n"
  "v_mov_b32 v115, 0\n"
  "s_waitcnt lgkmcnt(12)\n"
  "v_dot2c_f32_bf16 v112, s70, v48\n"
  "v_dot2c_f32_bf16 v113, s70, v49\n"
  "v_dot2c_f32_bf16 v114, s70, v50\n"
  "v_dot2c_f32_bf16 v115, s70, v51\n"
  "s_waitcnt lgkmcnt(10)\n"
  "v_dot2c_f32_bf16 v112, s70, v52\n"
  "v_dot2c_f32_bf16 v113, s70, v53\n"
  "v_dot2c_f32_bf16 v114, s70, v54\n"
  "v_dot2c_f32_bf16 v115, s70, v55\n"
  "s_waitcnt lgkmcnt(8)\n"
  "v_dot2c_f32_bf16 v112, s70, v56\n"
  "v_dot2c_f32_bf16 v113, s70, v57\n"
  "v_dot2c_f32_bf16 v114, s70, v58\n"
  "v_dot2c_f32_bf16 v115, s70, v59\n"
  "s_waitcnt lgkmcnt(6)\n"
  "v_dot2c_f32_bf16 v112, s70, v60\n"
  "v_dot2c_f32_bf16 v113, s70, v61\n"
  "v_dot2c_f32_bf16 v114, s70, v62\n"
  "v_dot2c_f32_bf16 v115, s70, v63\n"
  "s_waitcnt lgkmcnt(4)\n"
  "v_dot2c_f32_bf16 v112, s70, v64\n"
  "v_dot2c_f32_bf16 v113, s70, v65\n"
  "v_dot2c_f32_bf16 v114, s70, v66\n"
  "v_dot2c_f32_bf16 v115, s70, v67\n"
  "s_waitcnt lgkmcnt(2)\n"
  "v_dot2c_f32_bf16 v112, s70, v68\n"
  "v_dot2c_f32_bf16 v113, s70, v69\n"
  "v_dot2c_f32_bf16 v114, s70, v70\n"
  "v_dot2c_f32_bf16 v115, s70, v71\n"
  "s_waitcnt lgkmcnt(0)\n"
  "v_dot2c_f32_bf16 v112, s70, v72\n"
  "v_dot2c_f32_bf16 v113, s70, v73\n"
  "v_dot2c_f32_bf16 v114, s70, v74\n"
  "v_dot2c_f32_bf16 v115, s70, v75\n"
  "s_lshl_b32 s58, s52, 2\n"
  "v_add_u32 v7, s58, v102\n"
  "v_add_f32 v112, v112, v113\n"
  "v_add_f32 v114, v114, v115\n"
  "v_add_f32 v112, v112, v114\n"
  "v_mov_b32 v113, v112\n"
  "s_nop 1\n"
  "v_permlane32_swap_b32 v112, v113\n"
  "v_add_f32 v112, v112, v113\n"
  "v_cvt_pk_bf16_f32 v5, v112, v112\n"
  "v_and_b32 v5, 0xffff0000, v5\n"
  "v_sub_f32 v5, v112, v5\n"
  "v_cvt_pk_bf16_f32 v5, v112, v5\n"
  "ds_write_b32 v7, v5 offset:1024\n"
  "s_waitcnt lgkmcnt(0)\n"
  "s_barrier\n"
  "ds_read_b64_tr_b16 v[32:33], v99 offset:0\n"
  "ds_read_b64_tr_b16 v[34:35], v99 offset:512\n"
  "ds_read_b64_tr_b16 v[36:37], v100 offset:0\n"
  "ds_read_b64_tr_b16 v[38:39], v100 offset:512\n"
  "ds_read_b64_tr_b16 v[40:41], v101 offset:0\n"
  "ds_read_b64_tr_b16 v[42:43], v101 offset:512\n"
  "ds_read_b64_tr_b16 v[44:45], v99 offset:1024\n"
  "ds_read_b64_tr_b16 v[46:47], v99 offset:1536\n"
  "ds_read_b128 v[76:79], v102 offset:1024\n"
  "v_mfma_f32_32x32x16_bf16 v[16:31], v[48:51], v[128:131], 0\n"
  "v_mfma_f32_32x32x16_bf16 v[16:31], v[52:55], v[144:147], v[16:31]\n"
  "s_add_u32 m0, s73, 896\n"
  "s_nop 0\n"
  "buffer_load_dwordx4 v9, s[28:31], s89 offen offset:0 lds\n"
  "v_mfma_f32_32x32x16_bf16 v[16:31], v[56:59], v[160:163], v[16:31]\n"
  "s_waitcnt lgkmcnt(7)\n"
  "v_mfma_f32_32x32x16_bf16 v[16:31], v[32:35], v[132:135], v[16:31]\n"
  "ds_read_b64_tr_b16 v[32:33], v100 offset:1024\n"
  "ds_read_b64_tr_b16 v[34:35], v100 offset:1536\n"
  "s_add_u32 m0, s73, 1792\n"
  "s_nop 0\n"
  "buffer_load_dwordx4 v10, s[28:31], s89 offen offset:0 lds\n"
  "s_waitcnt lgkmcnt(7)\n"
  "v_mfma_f32_32x32x16_bf16 v[16:31], v[36:39], v[136:139], v[16:31]\n"
  "ds_read_b64_tr_b16 v[36:37], v101 offset:1024\n"
  "ds_read_b64_tr_b16 v[38:39], v101 offset:1536\n"
  "s_waitcnt lgkmcnt(7)\n"
  "v_mfma_f32_32x32x16_bf16 v[16:31], v[40:43], v[140:143], v[16:31]\n"
  "ds_read_b64_tr_b16 v[40:41], v99 offset:2048\n"
  "ds_read_b64_tr_b16 v[42:43], v99 offset:2560\n"
  "s_add_u32 m0, s73, 2688\n"
  "s_nop 0\n"
  "buffer_load_dwordx4 v11, s[28:31], s89 offen offset:0 lds\n"
  "s_waitcnt lgkmcnt(7)\n"
  "v_mfma_f32_32x32x16_bf16 v[16:31], v[44:47], v[148:151], v[16:31]\n"
  "ds_read_b64_tr_b16 v[44:45], v100 offset:2048\n"
  "ds_read_b64_tr_b16 v[46:47], v100 offset:2560\n"
  "v_mfma_f32_32x32x16_bf16 v[16:31], v[60:63], v[176:179], v[16:31]\n"
  "s_mul_i32 s89, s90, 229376\n"
  "s_add_u32 s89, s89, s88\n"
  "s_add_u32 s90, s90, 1\n"
  "s_cmp_eq_u32 s90, 7\n"
  "s_cselect_b32 s90, 0, s90\n"
  "s_add_u32 m0, s73, 3584\n"
  "s_nop 0\n"
  "buffer_load_dwordx4 v8, s[28:31], s89 offen offset:0 lds\n"
  "s_waitcnt lgkmcnt(6)\n"
  "v_mfma_f32_32x32x16_bf16 v[16:31], v[32:35], v[152:155], v[16:31]\n"
  "ds_read_b64_tr_b16 v[32:33], v101 offset:2048\n"
  "ds_read_b64_tr_b16 v[34:35], v101 offset:2560\n"
  "s_waitcnt lgkmcnt(6)\n"
  "v_mfma_f32_32x32x16_bf16 v[16:31], v[36:39], v[156:159], v[16:31]\n"
  "ds_read_b64_tr_b16 v[36:37], v99 offset:3072\n"
  "ds_read_b64_tr_b16 v[38:39], v99 offset:3584\n"
  "s_mov_b32 s63, 4096\n"
  "buffer_load_dwordx4 v[240:243], v94, s[12:15], s63 offen offset:0 sc1\n"
  "s_waitcnt lgkmcnt(6)\n"
  "v_mfma_f32_32x32x16_bf16 v[16:31], v[40:43], v[164:167], v[16:31]\n"
  "ds_read_b64_tr_b16 v[40:41], v100 offset:3072\n"
  "ds_read_b64_tr_b16 v[42:43], v100 offset:3584\n"
  "s_waitcnt lgkmcnt(6)\n"
  "v_mfma_f32_32x32x16_bf16 v[16:31], v[44:47], v[168:171], v[16:31]\n"
  "ds_read_b64_tr_b16 v[44:45], v101 offset:3072\n"
  "ds_read_b64_tr_b16 v[46:47], v101 offset:3584\n"
  "buffer_load_dwordx4 v[244:247], v94, s[12:15], s63 offen offset:16 sc1\n"
  "v_mfma_f32_32x32x16_bf16 v[16:31], v[64:67], v[192:195], v[16:31]\n"
  "s_waitcnt lgkmcnt(6)\n"
  "v_mfma_f32_32x32x16_bf16 v[16:31], v[32:35], v[172:175], v[16:31]\n"
  "ds_read_b64_tr_b16 v[32:33], v99 offset:4096\n"
  "ds_read_b64_tr_b16 v[34:35], v99 offset:4608\n"
  "s_add_u32 m0, s73, 4480\n"
  "s_nop 0\n"
  "buffer_load_dwordx4 v9, s[28:31], s89 offen offset:0 lds\n"
  "s_waitcnt lgkmcnt(6)\n"
  "v_mfma_f32_32x32x16_bf16 v[16:31], v[36:39], v[180:183], v[16:31]\n"
  "ds_read_b64_tr_b16 v[36:37], v100 offset:4096\n"
  "ds_read_b64_tr_b16 v[38:39], v100 offset:4608\n"
  "s_waitcnt lgkmcnt(6)\n"
  "v_mfma_f32_32x32x16_bf16 v[16:31], v[40:43], v[184:187], v[16:31]\n"
  "ds_read_b64_tr_b16 v[40:41], v101 offset:4096\n"
  "ds_read_b64_tr_b16 v[42:43], v101 offset:4608\n"
  "s_add_u32 m0, s73, 5376\n"
  "s_nop 0\n"
  "buffer_load_dwordx4 v10, s[28:31], s89 offen offset:0 lds\n"
  "s_waitcnt lgkmcnt(6)\n"
  "v_mfma_f32_32x32x16_bf16 v[16:31], v[44:47], v[188:191], v[16:31]\n"
  "ds_read_b64_tr_b16 v[44:45], v99 offset:5120\n"
  "ds_read_b64_tr_b16 v[46:47], v99 offset:5632\n"
  "v_mfma_f32_32x32x16_bf16 v[16:31], v[68:71], v[208:211], v[16:31]\n"
  "s_add_u32 m0, s73, 6272\n"
  "s_nop 0\n"
  "buffer_load_dwordx4 v11, s[28:31], s89 offen offset:0 lds\n"
  "s_waitcnt lgkmcnt(6)\n"
  "v_mfma_f32_32x32x16_bf16 v[16:31], v[32:35], v[196:199], v[16:31]\n"
  "ds_read_b64_tr_b16 v[32:33], v100 offset:5120\n"
  "ds_read_b64_tr_b16 v[34:35], v100 offset:5632\n"
  "s_waitcnt lgkmcnt(6)\n"
  "v_mfma_f32_32x32x16_bf16 v[16:31], v[36:39], v[200:203], v[16:31]\n"
  "ds_read_b64_tr_b16 v[36:37], v101 offset:5120\n"
  "ds_read_b64_tr_b16 v[38:39], v101 offset:5632\n"
  "s_mul_i32 s89, s90, 229376\n"
  "s_add_u32 s89, s89, s88\n"
  "s_add_u32 s90, s90, 1\n"
  "s_cmp_eq_u32 s90, 7\n"
  "s_cselect_b32 s90, 0, s90\n"
  "s_add_u32 m0, s73, 7168\n"
  "s_nop 0\n"
  "buffer_load_dwordx4 v8, s[28:31], s89 offen offset:0 lds\n"
  "s_waitcnt lgkmcnt(6)\n"
  "v_mfma_f32_32x32x16_bf16 v[16:31], v[40:43], v[204:207], v[16:31]\n"
  "ds_read_b64_tr_b16 v[40:41], v99 offset:6144\n"
  "ds_read_b64_tr_b16 v[42:43], v99 offset:6656\n"
  "s_waitcnt lgkmcnt(6)\n"
  "v_mfma_f32_32x32x16_bf16 v[16:31], v[44:47], v[212:215], v[16:31]\n"
  "ds_read_b64_tr_b16 v[44:45], v100 offset:6144\n"
  "ds_read_b64_tr_b16 v[46:47], v100 offset:6656\n"
  "s_add_u32 m0, s73, 8064\n"
  "s_nop 0\n"
  "buffer_load_dwordx4 v9, s[28:31], s89 offen offset:0 lds\n"
  "v_mfma_f32_32x32x16_bf16 v[16:31], v[72:75], v[224:227], v[16:31]\n"
  "s_waitcnt lgkmcnt(6)\n"
  "v_mfma_f32_32x32x16_bf16 v[16:31], v[32:35], v[216:219], v[16:31]\n"
  "ds_read_b64_tr_b16 v[32:33], v101 offset:6144\n"
  "ds_read_b64_tr_b16 v[34:35], v101 offset:6656\n"
  "s_add_u32 m0, s73, 8960\n"
  "s_nop 0\n"
  "buffer_load_dwordx4 v10, s[28:31], s89 offen offset:0 lds\n"
  "s_waitcnt lgkmcnt(6)\n"
  "v_mfma_f32_32x32x16_bf16 v[16:31], v[36:39], v[220:223], v[16:31]\n"
  "s_waitcnt lgkmcnt(4)\n"
  "v_mfma_f32_32x32x16_bf16 v[16:31], v[40:43], v[228:231], v[16:31]\n"
  "s_add_u32 m0, s73, 9856\n"
  "s_nop 0\n"
  "buffer_load_dwordx4 v11, s[28:31], s89 offen offset:0 lds\n"
  "s_waitcnt lgkmcnt(2)\n"
  "v_mfma_f32_32x32x16_bf16 v[16:31], v[44:47], v[232:235], v[16:31]\n"
  "s_waitcnt lgkmcnt(0)\n"
  "v_mfma_f32_32x32x16_bf16 v[16:31], v[32:35], v[236:239], v[16:31]\n"
  "s_mul_i32 s89, s90, 229376\n"
  "s_add_u32 s89, s89, s88\n"
  "s_add_u32 s90, s90, 1\n"
  "s_cmp_eq_u32 s90, 7\n"
  "s_cselect_b32 s90, 0, s90\n"
  "s_add_u32 m0, s73, 10752\n"
  "s_nop 0\n"
  "buffer_load_dwordx4 v8, s[28:31], s89 offen offset:0 lds\n"
  "s_waitcnt lgkmcnt(0)\n"
  "v_mfma_f32_32x32x16_bf16 v[16:31], v[76:79], v[124:127], v[16:31]\n"
  "s_lshl_b32 s58, s53, 10\n"
  "s_sub_u32 s92, s78, s58\n"
  "s_nop 9\n"
  "v_cvt_pk_bf16_f32 v48, v16, v17\n"
  "v_cvt_pk_bf16_f32 v49, v18, v19\n"
  "v_cvt_pk_bf16_f32 v50, v20, v21\n"
  "v_cvt_pk_bf16_f32 v51, v22, v23\n"
  "v_cvt_pk_bf16_f32 v52, v24, v25\n"
  "v_cvt_pk_bf16_f32 v53, v26, v27\n"
  "v_cvt_pk_bf16_f32 v54, v28, v29\n"
  "v_cvt_pk_bf16_f32 v55, v30, v31\n"
  "v_cndmask_b32 v54, v54, 0, s[74:75]\n"
  "v_cndmask_b32 v55, v55, 0, s[74:75]\n"
  "v_mfma_f32_32x32x16_bf16 v[56:71], v[116:119], v[48:51], 0\n"
  "s_nop 0\n"
  "v_mfma_f32_32x32x16_bf16 v[56:71], v[120:123], v[52:55], v[56:71]\n"
  "s_add_u32 s64, s53, 0\n"
  "s_and_b32 s64, s64, 3\n"
  "s_lshl_b32 s64, s64, 12\n"
  "s_add_u32 s64, s64, s92\n"
  "buffer_load_dwordx4 v[128:131], v2, s[12:15], s64 offen offset:0 sc1\n"
  "buffer_load_dwordx4 v[132:135], v2, s[12:15], s64 offen offset:1024 sc1\n"
  "buffer_load_dwordx4 v[136:139], v2, s[12:15], s64 offen offset:2048 sc1\n"
  "buffer_load_dwordx4 v[140:143], v2, s[12:15], s64 offen offset:3072 sc1\n"
  "s_add_u32 s64, s53, 1\n"
  "s_and_b32 s64, s64, 3\n"
  "s_lshl_b32 s64, s64, 12\n"
  "s_add_u32 s64, s64, s92\n"
  "buffer_load_dwordx4 v[144:147], v2, s[12:15], s64 offen offset:0 sc1\n"
  "buffer_load_dwordx4 v[148:151], v2, s[12:15], s64 offen offset:1024 sc1\n"
  "buffer_load_dwordx4 v[152:155], v2, s[12:15], s64 offen offset:2048 sc1\n"
  "buffer_load_dwordx4 v[156:159], v2, s[12:15], s64 offen offset:3072 sc1\n"
  ".Lf0:\n"
  "s_waitcnt vmcnt(16)\n"
  "v_cmp_ne_u64 s[58:59], s[44:45], v[240:241]\n"
  "v_cmp_ne_u64 s[60:61], s[44:45], v[242:243]\n"
  "v_cmp_ne_u64 vcc, s[44:45], v[244:245]\n"
  "s_or_b64 s[58:59], s[58:59], s[60:61]\n"
  "v_cmp_ne_u64 s[60:61], s[44:45], v[246:247]\n"
  "s_or_b64 vcc, vcc, s[58:59]\n"
  "s_or_b64 vcc, vcc, s[60:61]\n"
  "s_and_b64 vcc, vcc, s[48:49]\n"
  "s_cbranch_scc0 .Lf0_ok\n"
  "s_mov_b32 s77, 4096\n"
  "buffer_load_dwordx4 v[240:243], v94, s[12:15], s77 offen offset:0 sc1\n"
  "buffer_load_dwordx4 v[244:247], v94, s[12:15], s77 offen offset:16 sc1\n"
  "s_sub_u32 s76, s76, 1\n"
  "s_cselect_b64 s[48:49], 0, s[48:49]\n"
  "s_waitcnt vmcnt(0)\n"
  "s_branch .Lf0\n"
  ".Lf0_ok:\n"
  "v_mov_b32 v96, s44\n"
  "v_mov_b32 v97, s45\n"
  "v_or_b32 v6, %1, %2\n"
  "v_cmp_eq_u32 vcc, 0, v6\n"
  "v_mov_b32 v7, 2147483632\n"
  "s_nop 0\n"
  "v_cndmask_b32 v6, v7, 0, vcc\n"
  "buffer_store_dwordx2 v[96:97], v6, s[12:15], 0 offen sc1\n"
  "v_max3_f32 v106, v56, v57, v58\n"
  "v_max3_f32 v107, -v56, -v57, -v58\n"
  "v_max3_f32 v106, v106, v59, v60\n"
  "v_max3_f32 v107, v107, -v59, -v60\n"
  "v_max3_f32 v106, v106, v61, v62\n"
  "v_max3_f32 v107, v107, -v61, -v62\n"
  "v_max3_f32 v106, v106, v64, v65\n"
  "v_max3_f32 v107, v107, -v64, -v65\n"
  "v_max3_f32 v106, v106, v66, v67\n"
  "v_max3_f32 v107, v107, -v66, -v67\n"
  "v_max3_f32 v106, v106, v68, v69\n"
  "v_max3_f32 v107, v107, -v68, -v69\n"
  "s_add_u32 s64, s53, 2\n"
  "s_and_b32 s64, s64, 3\n"
  "s_lshl_b32 s64, s64, 12\n"
  "s_add_u32 s64, s64, s92\n"
  "buffer_load_dwordx4 v[160:163], v2, s[12:15], s64 offen offset:0 sc1\n"
  "v_max3_f32 v106, v106, v70, v70\n"
  "v_max3_f32 v107, v107, -v70, -v70\n"
  "s_nop 1\n"
  "v_permlane32_swap_b32 v106, v107\n"
  "v_max_f32 v106, v106, v107\n"
  "v_mul_f32 v82, -v106, v80\n"
  "v_mov_b32 v83, v82\n"
  "s_nop 1\n"
  "v_permlane32_swap_b32 v82, v83\n"
  "v_pk_fma_f32 v[16:17], v[56:57], v[80:81], v[82:83] op_sel_hi:[1,0,0]\n"
  "v_pk_fma_f32 v[56:57], v[56:57], v[80:81], v[82:83] op_sel:[0,0,1] op_sel_hi:[1,0,1] neg_lo:[1,0,0] neg_hi:[1,0,0]\n"
  "v_pk_fma_f32 v[18:19], v[58:59], v[80:81], v[82:83] op_sel_hi:[1,0,0]\n"
  "v_pk_fma_f32 v[58:59], v[58:59], v[80:81], v[82:83] op_sel:[0,0,1] op_sel_hi:[1,0,1] neg_lo:[1,0,0] neg_hi:[1,0,0]\n"
  "v_pk_fma_f32 v[20:21], v[60:61], v[80:81], v[82:83] op_sel_hi:[1,0,0]\n"
  "buffer_load_dwordx4 v[164:167], v2, s[12:15], s64 offen offset:1024 sc1\n"
  "v_pk_fma_f32 v[60:61], v[60:61], v[80:81], v[82:83] op_sel:[0,0,1] op_sel_hi:[1,0,1] neg_lo:[1,0,0] neg_hi:[1,0,0]\n"
  "v_pk_fma_f32 v[22:23], v[62:63], v[80:81], v[82:83] op_sel_hi:[1,0,0]\n"
  "v_pk_fma_f32 v[62:63], v[62:63], v[80:81], v[82:83] op_sel:[0,0,1] op_sel_hi:[1,0,1] neg_lo:[1,0,0] neg_hi:[1,0,0]\n"
  "v_pk_fma_f32 v[24:25], v[64:65], v[80:81], v[82:83] op_sel_hi:[1,0,0]\n"
  "v_pk_fma_f32 v[64:65], v[64:65], v[80:81], v[82:83] op_sel:[0,0,1] op_sel_hi:[1,0,1] neg_lo:[1,0,0] neg_hi:[1,0,0]\n"
  "v_pk_fma_f32 v[26:27], v[66:67], v[80:81], v[82:83] op_sel_hi:[1,0,0]\n"
  "v_pk_fma_f32 v[66:67], v[66:67], v[80:81], v[82:83] op_sel:[0,0,1] op_sel_hi:[1,0,1] neg_lo:[1,0,0] neg_hi:[1,0,0]\n"
  "v_pk_fma_f32 v[28:29], v[68:69], v[80:81], v[82:83] op_sel_hi:[1,0,0]\n"
  "v_pk_fma_f32 v[68:69], v[68:69], v[80:81], v[82:83] op_sel:[0,0,1] op_sel_hi:[1,0,1] neg_lo:[1,0,0] neg_hi:[1,0,0]\n"
  "v_pk_fma_f32 v[30:31], v[70:71], v[80:81], v[82:83] op_sel_hi:[1,0,0]\n"
  "v_pk_fma_f32 v[70:71], v[70:71], v[80:81], v[82:83] op_sel:[0,0,1] op_sel_hi:[1,0,1] neg_lo:[1,0,0] neg_hi:[1,0,0]\n"
  "v_exp_f32 v16, v16\n"
  "buffer_load_dwordx4 v[168:171], v2, s[12:15], s64 offen offset:2048 sc1\n"
  "v_exp_f32 v56, v56\n"
  "v_exp_f32 v17, v17\n"
  "v_exp_f32 v57, v57\n"
  "v_exp_f32 v18, v18\n"
  "v_exp_f32 v58, v58\n"
  "v_exp_f32 v19, v19\n"
  "v_exp_f32 v59, v59\n"
  "v_exp_f32 v20, v20\n"
  "v_exp_f32 v60, v60\n"
  "v_exp_f32 v21, v21\n"
  "v_exp_f32 v61, v61\n"
  "v_exp_f32 v22, v22\n"
  "buffer_load_dwordx4 v[172:175], v2, s[12:15], s64 offen offset:3072 sc1\n"
  "v_exp_f32 v62, v62\n"
  "v_mov_b32 v23, 0\n"
  "v_mov_b32 v63, 0\n"
  "v_exp_f32 v24, v24\n"
  "v_exp_f32 v64, v64\n"
  "v_exp_f32 v25, v25\n"
  "v_exp_f32 v65, v65\n"
  "v_exp_f32 v26, v26\n"
  "v_exp_f32 v66, v66\n"
  "v_exp_f32 v27, v27\n"
  "v_exp_f32 v67, v67\n"
  "v_exp_f32 v28, v28\n"
  "s_add_u32 s64, s53, 3\n"
  "s_and_b32 s64, s64, 3\n"
  "s_lshl_b32 s64, s64, 12\n"
  "s_add_u32 s64, s64, s92\n"
  "buffer_load_dwordx4 v[176:179], v2, s[12:15], s64 offen offset:0 sc1\n"
  "v_exp_f32 v68, v68\n"
  "v_exp_f32 v29, v29\n"
  "v_exp_f32 v69, v69\n"
  "v_exp_f32 v30, v30\n"
  "v_exp_f32 v70, v70\n"
  "v_mov_b32 v31, 0\n"
  "v_mov_b32 v71, 0\n"
  "v_pk_add_f32 v[82:83], v[16:17], v[18:19]\n"
  "v_pk_add_f32 v[84:85], v[56:57], v[58:59]\n"
  "v_pk_add_f32 v[82:83], v[82:83], v[20:21]\n"
  "v_pk_add_f32 v[84:85], v[84:85], v[60:61]\n"
  "v_pk_add_f32 v[82:83], v[82:83], v[22:23]\n"
  "buffer_load_dwordx4 v[180:183], v2, s[12:15], s64 offen offset:1024 sc1\n"
  "v_pk_add_f32 v[84:85], v[84:85], v[62:63]\n"
  "v_pk_add_f32 v[82:83], v[82:83], v[24:25]\n"
  "v_pk_add_f32 v[84:85], v[84:85], v[64:65]\n"
  "v_pk_add_f32 v[82:83], v[82:83], v[26:27]\n"
  "v_pk_add_f32 v[84:85], v[84:85], v[66:67]\n"
  "v_pk_add_f32 v[82:83], v[82:83], v[28:29]\n"
  "v_pk_add_f32 v[84:85], v[84:85], v[68:69]\n"
  "v_pk_add_f32 v[82:83], v[82:83], v[30:31]\n"
  "v_pk_add_f32 v[84:85], v[84:85], v[70:71]\n"
  "v_add_f32 v106, v82, v83\n"
  "v_add_f32 v107, v84, v85\n"
  "s_nop 1\n"
  "v_permlane32_swap_b32 v106, v107\n"
  "buffer_load_dwordx4 v[184:187], v2, s[12:15], s64 offen offset:2048 sc1\n"
  "v_add_f32 v106, v106, v107\n"
  "v_rcp_f32 v86, v106\n"
  "s_nop 0\n"
  "v_mov_b32 v87, v86\n"
  "s_nop 1\n"
  "v_permlane32_swap_b32 v86, v87\n"
  "v_pk_mul_f32 v[16:17], v[16:17], v[86:87] op_sel_hi:[1,0]\n"
  "v_pk_mul_f32 v[56:57], v[56:57], v[86:87] op_sel:[0,1] op_sel_hi:[1,1]\n"
  "v_pk_mul_f32 v[18:19], v[18:19], v[86:87] op_sel_hi:[1,0]\n"
  "v_pk_mul_f32 v[58:59], v[58:59], v[86:87] op_sel:[0,1] op_sel_hi:[1,1]\n"
  "v_pk_mul_f32 v[20:21], v[20:21], v[86:87] op_sel_hi:[1,0]\n"
  "v_pk_mul_f32 v[60:61], v[60:61], v[86:87] op_sel:[0,1] op_sel_hi:[1,1]\n"
  "v_pk_mul_f32 v[22:23], v[22:23], v[86:87] op_sel_hi:[1,0]\n"
  "v_pk_mul_f32 v[62:63], v[62:63], v[86:87] op_sel:[0,1] op_sel_hi:[1,1]\n"
  "buffer_load_dwordx4 v[188:191], v2, s[12:15], s64 offen offset:3072 sc1\n"
  "v_pk_mul_f32 v[24:25], v[24:25], v[86:87] op_sel_hi:[1,0]\n"
  "v_pk_mul_f32 v[64:65], v[64:65], v[86:87] op_sel:[0,1] op_sel_hi:[1,1]\n"
  "v_pk_mul_f32 v[26:27], v[26:27], v[86:87] op_sel_hi:[1,0]\n"
  "v_pk_mul_f32 v[66:67], v[66:67], v[86:87] op_sel:[0,1] op_sel_hi:[1,1]\n"
  "v_pk_mul_f32 v[28:29], v[28:29], v[86:87] op_sel_hi:[1,0]\n"
  "v_pk_mul_f32 v[68:69], v[68:69], v[86:87] op_sel:[0,1] op_sel_hi:[1,1]\n"
  "v_pk_mul_f32 v[30:31], v[30:31], v[86:87] op_sel_hi:[1,0]\n"
  "v_pk_mul_f32 v[70:71], v[70:71], v[86:87] op_sel:[0,1] op_sel_hi:[1,1]\n"
  "v_cvt_pk_bf16_f32 v240, v16, v17\n"
  "v_cvt_pk_bf16_f32 v248, v56, v57\n"
  "v_cvt_pk_bf16_f32 v241, v18, v19\n"
  "v_cvt_pk_bf16_f32 v249, v58, v59\n"
  "s_add_u32 s66, s57, 0\n"
  "s_and_b32 s66, s66, 3\n"
  "s_lshl_b32 s66, s66, 12\n"
  "s_add_u32 s66, s66, s91\n"
  "buffer_load_dwordx4 a[160:163], v12, s[12:15], s66 offen offset:0 sc1\n"
  "v_cvt_pk_bf16_f32 v242, v20, v21\n"
  "v_cvt_pk_bf16_f32 v250, v60, v61\n"
  "v_cvt_pk_bf16_f32 v243, v22, v23\n"
  "v_cvt_pk_bf16_f32 v251, v62, v63\n"
  "v_cvt_pk_bf16_f32 v244, v24, v25\n"
  "v_cvt_pk_bf16_f32 v252, v64, v65\n"
  "v_cvt_pk_bf16_f32 v245, v26, v27\n"
  "v_cvt_pk_bf16_f32 v253, v66, v67\n"
  "v_cvt_pk_bf16_f32 v246, v28, v29\n"
  "v_cvt_pk_bf16_f32 v254, v68, v69\n"
  "v_cvt_pk_bf16_f32 v247, v30, v31\n"
  "v_cvt_pk_bf16_f32 v255, v70, v71\n"
  "buffer_load_dwordx4 a[164:167], v13, s[12:15], s66 offen offset:0 sc1\n"
  "s_waitcnt vmcnt(15)\n"
  "v_xor_b32 v5, s100, v131\n"
  "v_bitop3_b32 v5, v135, s100, v5 bitop3:0xbe\n"
  "v_bitop3_b32 v5, v139, s100, v5 bitop3:0xbe\n"
  "v_bitop3_b32 v5, v143, s100, v5 bitop3:0xbe\n"
  "v_cmp_lt_u32 vcc, 0xffff, v5\n"
  "s_cbranch_vccnz .La0_f\n"
  ".La0_k:\n"
  "v_lshlrev_b32 v16, 16, v128\n"
  "v_and_b32 v17, 0xffff0000, v128\n"
  "v_lshlrev_b32 v18, 16, v129\n"
  "v_and_b32 v19, 0xffff0000, v129\n"
  "v_lshlrev_b32 v20, 16, v130\n"
  "v_and_b32 v21, 0xffff0000, v130\n"
  "v_lshlrev_b32 v22, 16, v131\n"
  "v_and_b32 v23, 0xffff0000, v131\n"
  "v_dot2c_f32_bf16 v16, s71, v132\n"
  "v_dot2c_f32_bf16 v17, s72, v132\n"
  "v_dot2c_f32_bf16 v18, s71, v133\n"
  "v_dot2c_f32_bf16 v19, s72, v133\n"
  "v_dot2c_f32_bf16 v20, s71, v134\n"
  "v_dot2c_f32_bf16 v21, s72, v134\n"
  "v_dot2c_f32_bf16 v22, s71, v135\n"
  "v_dot2c_f32_bf16 v23, s72, v135\n"
  "buffer_load_dwordx4 a[168:171], v12, s[12:15], s66 offen offset:2048 sc1\n"
  "v_dot2c_f32_bf16 v16, s71, v136\n"
  "v_dot2c_f32_bf16 v17, s72, v136\n"
  "v_dot2c_f32_bf16 v18, s71, v137\n"
  "v_dot2c_f32_bf16 v19, s72, v137\n"
  "v_dot2c_f32_bf16 v20, s71, v138\n"
  "v_dot2c_f32_bf16 v21, s72, v138\n"
  "v_dot2c_f32_bf16 v22, s71, v139\n"
  "v_dot2c_f32_bf16 v23, s72, v139\n"
  "v_dot2c_f32_bf16 v16, s71, v140\n"
  "v_dot2c_f32_bf16 v17, s72, v140\n"
  "v_dot2c_f32_bf16 v18, s71, v141\n"
  "v_dot2c_f32_bf16 v19, s72, v141\n"
  "v_dot2c_f32_bf16 v20, s71, v142\n"
  "v_dot2c_f32_bf16 v21, s72, v142\n"
  "v_dot2c_f32_bf16 v22, s71, v143\n"
  "v_dot2c_f32_bf16 v23, s72, v143\n"
  "buffer_load_dwordx4 a[172:175], v13, s[12:15], s66 offen offset:2048 sc1\n"
  "s_waitcnt vmcnt(13)\n"
  "v_xor_b32 v5, s100, v147\n"
  "v_bitop3_b32 v5, v151, s100, v5 bitop3:0xbe\n"
  "v_bitop3_b32 v5, v155, s100, v5 bitop3:0xbe\n"
  "v_bitop3_b32 v5, v159, s100, v5 bitop3:0xbe\n"
  "v_cmp_lt_u32 vcc, 0xffff, v5\n"
  "s_cbranch_vccnz .La1_f\n"
  ".La1_k:\n"
  "v_dot2c_f32_bf16 v16, s71, v144\n"
  "v_dot2c_f32_bf16 v17, s72, v144\n"
  "v_dot2c_f32_bf16 v18, s71, v145\n"
  "v_dot2c_f32_bf16 v19, s72, v145\n"
  "v_dot2c_f32_bf16 v20, s71, v146\n"
  "v_dot2c_f32_bf16 v21, s72, v146\n"
  "v_dot2c_f32_bf16 v22, s71, v147\n"
  "v_dot2c_f32_bf16 v23, s72, v147\n"
  "v_dot2c_f32_bf16 v16, s71, v148\n"
  "v_dot2c_f32_bf16 v17, s72, v148\n"
  "v_dot2c_f32_bf16 v18, s71, v149\n"
  "v_dot2c_f32_bf16 v19, s72, v149\n"
  "v_dot2c_f32_bf16 v20, s71, v150\n"
  "v_dot2c_f32_bf16 v21, s72, v150\n"
  "v_dot2c_f32_bf16 v22, s71, v151\n"
  "v_dot2c_f32_bf16 v23, s72, v151\n"
  "s_add_u32 s66, s57, 1\n"
  "s_and_b32 s66, s66, 3\n"
  "s_lshl_b32 s66, s66, 12\n"
  "s_add_u32 s66, s66, s91\n"
  "buffer_load_dwordx4 a[176:179], v12, s[12:15], s66 offen offset:0 sc1\n"
  "v_dot2c_f32_bf16 v16, s71, v152\n"
  "v_dot2c_f32_bf16 v17, s72, v152\n"
  "v_dot2c_f32_bf16 v18, s71, v153\n"
  "v_dot2c_f32_bf16 v19, s72, v153\n"
  "v_dot2c_f32_bf16 v20, s71, v154\n"
  "v_dot2c_f32_bf16 v21, s72, v154\n"
  "v_dot2c_f32_bf16 v22, s71, v155\n"
  "v_dot2c_f32_bf16 v23, s72, v155\n"
  "v_dot2c_f32_bf16 v16, s71, v156\n"
  "v_dot2c_f32_bf16 v17, s72, v156\n"
  "v_dot2c_f32_bf16 v18, s71, v157\n"
  "v_dot2c_f32_bf16 v19, s72, v157\n"
  "v_dot2c_f32_bf16 v20, s71, v158\n"
  "v_dot2c_f32_bf16 v21, s72, v158\n"
  "v_dot2c_f32_bf16 v22, s71, v159\n"
  "v_dot2c_f32_bf16 v23, s72, v159\n"
  "buffer_load_dwordx4 a[180:183], v13, s[12:15], s66 offen offset:0 sc1\n"
  "s_waitcnt vmcnt(10)\n"
  "v_xor_b32 v5, s100, v163\n"
  "v_bitop3_b32 v5, v167, s100, v5 bitop3:0xbe\n"
  "v_bitop3_b32 v5, v171, s100, v5 bitop3:0xbe\n"
  "v_bitop3_b32 v5, v175, s100, v5 bitop3:0xbe\n"
  "v_cmp_lt_u32 vcc, 0xffff, v5\n"
  "s_cbranch_vccnz .La2_f\n"
  ".La2_k:\n"
  "v_dot2c_f32_bf16 v16, s71, v160\n"
  "v_dot2c_f32_bf16 v17, s72, v160\n"
  "v_dot2c_f32_bf16 v18, s71, v161\n"
  "v_dot2c_f32_bf16 v19, s72, v161\n"
  "v_dot2c_f32_bf16 v20, s71, v162\n"
  "v_dot2c_f32_bf16 v21, s72, v162\n"
  "v_dot2c_f32_bf16 v22, s71, v163\n"
  "v_dot2c_f32_bf16 v23, s72, v163\n"
  "v_dot2c_f32_bf16 v16, s71, v164\n"
  "v_dot2c_f32_bf16 v17, s72, v164\n"
  "v_dot2c_f32_bf16 v18, s71, v165\n"
  "v_dot2c_f32_bf16 v19, s72, v165\n"
  "v_dot2c_f32_bf16 v20, s71, v166\n"
  "v_dot2c_f32_bf16 v21, s72, v166\n"
  "v_dot2c_f32_bf16 v22, s71, v167\n"
  "v_dot2c_f32_bf16 v23, s72, v167\n"
  "buffer_load_dwordx4 a[184:187], v12, s[12:15], s66 offen offset:2048 sc1\n"
  "v_dot2c_f32_bf16 v16, s71, v168\n"
  "v_dot2c_f32_bf16 v17, s72, v168\n"
  "v_dot2c_f32_bf16 v18, s71, v169\n"
  "v_dot2c_f32_bf16 v19, s72, v169\n"
  "v_dot2c_f32_bf16 v20, s71, v170\n"
  "v_dot2c_f32_bf16 v21, s72, v170\n"
  "v_dot2c_f32_bf16 v22, s71, v171\n"
  "v_dot2c_f32_bf16 v23, s72, v171\n"
  "v_dot2c_f32_bf16 v16, s71, v172\n"
  "v_dot2c_f32_bf16 v17, s72, v172\n"
  "v_dot2c_f32_bf16 v18, s71, v173\n"
  "v_dot2c_f32_bf16 v19, s72, v173\n"
  "v_dot2c_f32_bf16 v20, s71, v174\n"
  "v_dot2c_f32_bf16 v21, s72, v174\n"
  "v_dot2c_f32_bf16 v22, s71, v175\n"
  "v_dot2c_f32_bf16 v23, s72, v175\n"
  "buffer_load_dwordx4 a[188:191], v13, s[12:15], s66 offen offset:2048 sc1\n"
  "s_waitcnt vmcnt(8)\n"
  "v_xor_b32 v5, s100, v179\n"
  "v_bitop3_b32 v5, v183, s100, v5 bitop3:0xbe\n"
  "v_bitop3_b32 v5, v187, s100, v5 bitop3:0xbe\n"
  "v_bitop3_b32 v5, v191, s100, v5 bitop3:0xbe\n"
  "v_cmp_lt_u32 vcc, 0xffff, v5\n"
  "s_cbranch_vccnz .La3_f\n"
  ".La3_k:\n"
  "v_dot2c_f32_bf16 v16, s71, v176\n"
  "v_dot2c_f32_bf16 v17, s72, v176\n"
  "v_dot2c_f32_bf16 v18, s71, v177\n"
  "v_dot2c_f32_bf16 v19, s72, v177\n"
  "v_dot2c_f32_bf16 v20, s71, v178\n"
  "v_dot2c_f32_bf16 v21, s72, v178\n"
  "v_dot2c_f32_bf16 v22, s71, v179\n"
  "v_dot2c_f32_bf16 v23, s72, v179\n"
  "v_dot2c_f32_bf16 v16, s71, v180\n"
  "v_dot2c_f32_bf16 v17, s72, v180\n"
  "v_dot2c_f32_bf16 v18, s71, v181\n"
  "v_dot2c_f32_bf16 v19, s72, v181\n"
  "v_dot2c_f32_bf16 v20, s71, v182\n"
  "v_dot2c_f32_bf16 v21, s72, v182\n"
  "v_dot2c_f32_bf16 v22, s71, v183\n"
  "v_dot2c_f32_bf16 v23, s72, v183\n"
  "s_add_u32 s66, s57, 2\n"
  "s_and_b32 s66, s66, 3\n"
  "s_lshl_b32 s66, s66, 12\n"
  "s_add_u32 s66, s66, s91\n"
  "buffer_load_dwordx4 a[192:195], v12, s[12:15], s66 offen offset:0 sc1\n"
  "v_dot2c_f32_bf16 v16, s71, v184\n"
  "v_dot2c_f32_bf16 v17, s72, v184\n"
  "v_dot2c_f32_bf16 v18, s71, v185\n"
  "v_dot2c_f32_bf16 v19, s72, v185\n"
  "v_dot2c_f32_bf16 v20, s71, v186\n"
  "v_dot2c_f32_bf16 v21, s72, v186\n"
  "v_dot2c_f32_bf16 v22, s71, v187\n"
  "v_dot2c_f32_bf16 v23, s72, v187\n"
  "v_dot2c_f32_bf16 v16, s71, v188\n"
  "v_dot2c_f32_bf16 v17, s72, v188\n"
  "v_dot2c_f32_bf16 v18, s71, v189\n"
  "v_dot2c_f32_bf16 v19, s72, v189\n"
  "v_dot2c_f32_bf16 v20, s71, v190\n"
  "v_dot2c_f32_bf16 v21, s72, v190\n"
  "v_dot2c_f32_bf16 v22, s71, v191\n"
  "v_dot2c_f32_bf16 v23, s72, v191\n"
  "buffer_load_dwordx4 a[196:199], v13, s[12:15], s66 offen offset:0 sc1\n"
  "s_lshl_b32 s58, s52, 10\n"
  "s_add_u32 s58, s58, 150016\n"
  "v_add_u32 v6, s58, v2\n"
  "v_and_b32 v7, 32, v1\n"
  "v_add_u32 v7, v7, v108\n"
  "v_lshlrev_b32 v7, 4, v7\n"
  "v_add_u32 v7, 150016, v7\n"
  "v_cvt_pk_bf16_f32 v24, v16, v17\n"
  "v_cvt_pk_bf16_f32 v25, v18, v19\n"
  "v_cvt_pk_bf16_f32 v26, v20, v21\n"
  "v_cvt_pk_bf16_f32 v27, v22, v23\n"
  "ds_write_b128 v6, v[24:27]\n"
  "v_mov_b32 v106, s44\n"
  "v_mov_b32 v107, s45\n"
  "v_cmp_eq_u32 vcc, 0, %2\n"
  "v_mov_b32 v5, 2147483632\n"
  "s_lshl_b32 s58, %1, 3\n"
  "v_cndmask_b32 v5, v5, 0, vcc\n"
  "s_add_u32 s58, s58, 6144\n"
  "s_waitcnt lgkmcnt(0)\n"
  "s_barrier\n"
  "buffer_store_dwordx2 v[106:107], v5, s[12:15], s58 offen sc1\n"
  "ds_read_b128 v[32:35], v7 offset:0\n"
  "ds_read_b128 v[36:39], v7 offset:1024\n"
  "ds_read_b128 v[40:43], v7 offset:2048\n"
  "ds_read_b128 v[44:47], v7 offset:3072\n"
  "s_waitcnt lgkmcnt(3)\n"
  "v_mfma_f32_32x32x16_bf16 v[48:63], v[32:35], v[240:243], 0\n"
  "s_waitcnt lgkmcnt(2)\n"
  "v_mfma_f32_32x32x16_bf16 v[48:63], v[36:39], v[244:247], v[48:63]\n"
  "s_waitcnt lgkmcnt(1)\n"
  "v_mfma_f32_32x32x16_bf16 v[48:63], v[40:43], v[248:251], v[48:63]\n"
  "s_waitcnt lgkmcnt(0)\n"
  "v_mfma_f32_32x32x16_bf16 v[48:63], v[44:47], v[252:255], v[48:63]\n"
  "s_lshl_b32 s58, s83, 2\n"
  "s_add_u32 s58, s58, s52\n"
  "s_lshl_b32 s58, s58, 13\n"
  "s_add_u32 s58, s58, 3579904\n"
  "s_add_u32 s92, s58, s3\n"
  "s_nop 6\n"
  "v_mov_b32 v106, v63\n"
  "v_mov_b32 v107, v63\n"
  "s_nop 1\n"
  "v_permlane32_swap_b32 v106, v107\n"
  "v_add_f32 v107, 0x358637bd, v107\n"
  "v_rcp_f32 v106, v107\n"
  "s_nop 0\n"
  "v_pk_mul_f32 v[48:49], v[48:49], v[106:107] op_sel_hi:[1,0]\n"
  "v_pk_mul_f32 v[50:51], v[50:51], v[106:107] op_sel_hi:[1,0]\n"
  "v_pk_mul_f32 v[52:53], v[52:53], v[106:107] op_sel_hi:[1,0]\n"
  "v_pk_mul_f32 v[54:55], v[54:55], v[106:107] op_sel_hi:[1,0]\n"
  "v_pk_mul_f32 v[56:57], v[56:57], v[106:107] op_sel_hi:[1,0]\n"
  "v_pk_mul_f32 v[58:59], v[58:59], v[106:107] op_sel_hi:[1,0]\n"
  "v_pk_mul_f32 v[60:61], v[60:61], v[106:107] op_sel_hi:[1,0]\n"
  "v_pk_mul_f32 v[62:63], v[62:63], v[106:107] op_sel_hi:[1,0]\n"
  "v_cvt_pk_bf16_f32 v64, v48, v49\n"
  "v_cvt_pk_bf16_f32 v65, v50, v51\n"
  "v_cvt_pk_bf16_f32 v66, v52, v53\n"
  "v_cvt_pk_bf16_f32 v67, v54, s100\n"
  "buffer_store_dwordx4 v[64:67], v2, s[12:15], s79 offen offset:0 sc1\n"
  "v_cvt_pk_bf16_f32 v68, v56, v57\n"
  "v_cvt_pk_bf16_f32 v69, v58, v59\n"
  "v_cvt_pk_bf16_f32 v70, v60, v61\n"
  "v_cvt_pk_bf16_f32 v71, v62, s100\n"
  "buffer_store_dwordx4 v[68:71], v2, s[12:15], s79 offen offset:1024 sc1\n"
  "s_cmp_eq_u32 s3, 0\n"
  "s_cselect_b32 s100, s100, 0\n"
  "buffer_load_dwordx4 a[200:203], v12, s[12:15], s66 offen offset:2048 sc1\n"
  "buffer_load_dwordx4 a[204:207], v13, s[12:15], s66 offen offset:2048 sc1\n"
  "s_add_u32 s66, s57, 3\n"
  "s_and_b32 s66, s66, 3\n"
  "s_lshl_b32 s66, s66, 12\n"
  "s_add_u32 s66, s66, s91\n"
  "buffer_load_dwordx4 a[208:211], v12, s[12:15], s66 offen offset:0 sc1\n"
  "buffer_load_dwordx4 a[212:215], v13, s[12:15], s66 offen offset:0 sc1\n"
  "buffer_load_dwordx4 a[216:219], v12, s[12:15], s66 offen offset:2048 sc1\n"
  "buffer_load_dwordx4 a[220:223], v13, s[12:15], s66 offen offset:2048 sc1\n"
  "s_add_u32 m0, s73, 11648\n"
  "s_nop 0\n"
  "buffer_load_dwordx4 v9, s[28:31], s89 offen offset:0 lds\n"
  "s_add_u32 m0, s73, 12544\n"
  "s_nop 0\n"
  "buffer_load_dwordx4 v10, s[28:31], s89 offen offset:0 lds\n"
  "s_add_u32 m0, s73, 13440\n"
  "s_nop 0\n"
  "buffer_load_dwordx4 v11, s[28:31], s89 offen offset:0 lds\n"
  "s_mul_i32 s89, s90, 229376\n"
  "s_add_u32 s89, s89, s88\n"
  "s_add_u32 s90, s90, 1\n"
  "s_cmp_eq_u32 s90, 7\n"
  "s_cselect_b32 s90, 0, s90\n"
  "s_add_u32 m0, s73, 14336\n"
  "s_nop 0\n"
  "buffer_load_dwordx4 v8, s[28:31], s89 offen offset:0 lds\n"
  "s_add_u32 m0, s73, 15232\n"
  "s_nop 0\n"
  "buffer_load_dwordx4 v9, s[28:31], s89 offen offset:0 lds\n"
  "v_and_b32 v205, 31, v1\n"
  "v_lshlrev_b32 v206, 1, v205\n"
  "v_mul_u32_u24 v205, 896, v205\n"
  "v_lshrrev_b32 v207, 5, v1\n"
  "v_lshlrev_b32 v207, 4, v207\n"
  "v_add_u32 v205, v205, v207\n"
  "s_lshl_b32 s63, s84, 7\n"
  "s_lshl_b32 s62, s52, 5\n"
  "s_add_u32 s63, s63, s62\n"
  "s_mul_i32 s62, s83, 28672\n"
  "s_add_u32 s62, s62, s63\n"
  "s_add_u32 s99, s62, s3\n"
  "buffer_load_dwordx4 v[192:195], v205, s[4:7], s99 offen\n"
  "s_add_u32 s62, s99, 28672\n"
  "buffer_load_dwordx4 v[196:199], v205, s[4:7], s62 offen\n"
  "buffer_load_dwordx4 v[200:203], v207, s[36:39], s63 offen\n"
  "s_mul_i32 s62, s54, 224\n"
  "s_lshl_b32 s63, s52, 6\n"
  "s_add_u32 s62, s62, s63\n"
  "buffer_load_ushort v204, v206, s[32:35], s62 offen\n"
  "v_add_u32 v206, 0x8000, v2\n"
  "s_add_u32 m0, s73, 16128\n"
  "s_nop 0\n"
  "buffer_load_dwordx4 v10, s[28:31], s89 offen offset:0 lds\n"
  "s_add_u32 m0, s73, 17024\n"
  "s_nop 0\n"
  "buffer_load_dwordx4 v11, s[28:31], s89 offen offset:0 lds\n"
  "s_mul_i32 s89, s90, 229376\n"
  "s_add_u32 s89, s89, s88\n"
  "s_add_u32 s90, s90, 1\n"
  "s_cmp_eq_u32 s90, 7\n"
  "s_cselect_b32 s90, 0, s90\n"
  "s_add_u32 m0, s73, 17920\n"
  "s_nop 0\n"
  "buffer_load_dwordx4 v8, s[28:31], s89 offen offset:0 lds\n"
  "s_add_u32 m0, s73, 18816\n"
  "s_nop 0\n"
  "buffer_load_dwordx4 v9, s[28:31], s89 offen offset:0 lds\n"
  "s_add_u32 m0, s73, 19712\n"
  "s_nop 0\n"
  "buffer_load_dwordx4 v10, s[28:31], s89 offen offset:0 lds\n"
  "s_add_u32 m0, s73, 20608\n"
  "s_nop 0\n"
  "buffer_load_dwordx4 v11, s[28:31], s89 offen offset:0 lds\n"
  "s_mul_i32 s89, s90, 229376\n"
  "s_add_u32 s89, s89, s88\n"
  "s_add_u32 s90, s90, 1\n"
  "s_cmp_eq_u32 s90, 7\n"
  "s_cselect_b32 s90, 0, s90\n"
  "s_add_u32 m0, s73, 21504\n"
  "s_nop 0\n"
  "buffer_load_dwordx4 v8, s[28:31], s89 offen offset:0 lds\n"
  "s_add_u32 m0, s73, 22400\n"
  "s_nop 0\n"
  "buffer_load_dwordx4 v9, s[28:31], s89 offen offset:0 lds\n"
  "s_add_u32 m0, s73, 23296\n"
  "s_nop 0\n"
  "buffer_load_dwordx4 v10, s[28:31], s89 offen offset:0 lds\n"
  "s_add_u32 m0, s73, 24192\n"
  "s_nop 0\n"
  "buffer_load_dwordx4 v11, s[28:31], s89 offen offset:0 lds\n"
  "s_add_u32 s67, s57, 0\n"
  "s_and_b32 s67, s67, 3\n"
  "s_mul_i32 s67, s67, 14336\n"
  "s_add_u32 s67, s67, s96\n"
  "buffer_load_dwordx4 a[0:3], v12, s[12:15], s67 offen offset:0 sc1\n"
  "buffer_load_dwordx4 a[4:7], v13, s[12:15], s67 offen offset:0 sc1\n"
  "buffer_load_dwordx4 a[32:35], v12, s[12:15], s67 offen offset:2048 sc1\n"
  "buffer_load_dwordx4 a[36:39], v13, s[12:15], s67 offen offset:2048 sc1\n"
  "s_bitcmp1_b32 0x2a54, s54\n"
  "s_cbranch_scc1 .Lq1\n"
  "s_add_u32 s67, s57, 1\n"
  "s_and_b32 s67, s67, 3\n"
  "s_mul_i32 s67, s67, 14336\n"
  "s_add_u32 s67, s67, s96\n"
  "buffer_load_dwordx4 a[8:11], v12, s[12:15], s67 offen offset:0 sc1\n"
  "buffer_load_dwordx4 a[12:15], v13, s[12:15], s67 offen offset:0 sc1\n"
  "buffer_load_dwordx4 a[40:43], v12, s[12:15], s67 offen offset:2048 sc1\n"
  "buffer_load_dwordx4 a[44:47], v13, s[12:15], s67 offen offset:2048 sc1\n"
  "s_add_u32 s67, s57, 2\n"
  "s_and_b32 s67, s67, 3\n"
  "s_mul_i32 s67, s67, 14336\n"
  "s_add_u32 s67, s67, s96\n"
  "buffer_load_dwordx4 a[16:19], v12, s[12:15], s67 offen offset:0 sc1\n"
  "buffer_load_dwordx4 a[20:23], v13, s[12:15], s67 offen offset:0 sc1\n"
  "buffer_load_dwordx4 a[48:51], v12, s[12:15], s67 offen offset:2048 sc1\n"
  "buffer_load_dwordx4 a[52:55], v13, s[12:15], s67 offen offset:2048 sc1\n"
  "s_add_u32 s67, s57, 3\n"
  "s_and_b32 s67, s67, 3\n"
  "s_mul_i32 s67, s67, 14336\n"
  "s_add_u32 s67, s67, s96\n"
  "buffer_load_dwordx4 a[24:27], v12, s[12:15], s67 offen offset:0 sc1\n"
  "buffer_load_dwordx4 a[28:31], v13, s[12:15], s67 offen offset:0 sc1\n"
  "buffer_load_dwordx4 a[56:59], v12, s[12:15], s67 offen offset:2048 sc1\n"
  "buffer_load_dwordx4 a[60:63], v13, s[12:15], s67 offen offset:2048 sc1\n"
  "s_add_u32 s67, s57, 0\n"
  "s_and_b32 s67, s67, 3\n"
  "s_mul_i32 s67, s67, 14336\n"
  "s_add_u32 s67, s67, s96\n"
  "s_add_u32 s67, s67, 4096\n"
  "buffer_load_dwordx4 a[64:67], v12, s[12:15], s67 offen offset:0 sc1\n"
  "buffer_load_dwordx4 a[68:71], v13, s[12:15], s67 offen offset:0 sc1\n"
  "buffer_load_dwordx4 a[96:99], v12, s[12:15], s67 offen offset:2048 sc1\n"
  "buffer_load_dwordx4 a[100:103], v13, s[12:15], s67 offen offset:2048 sc1\n"
  ".Lq1:\n"
  "s_add_u32 s62, s57, 0\n"
  "s_and_b32 s62, s62, 3\n"
  "s_lshl_b32 s62, s62, 11\n"
  "s_add_u32 s62, s62, s92\n"
  "buffer_load_dwordx4 v[128:131], v2, s[12:15], s62 offen offset:0 sc1\n"
  "buffer_load_dwordx4 v[132:135], v2, s[12:15], s62 offen offset:1024 sc1\n"
  "buffer_load_dwordx4 v[136:139], v206, s[12:15], s62 offen offset:0 sc1\n"
  "buffer_load_dwordx4 v[140:143], v206, s[12:15], s62 offen offset:1024 sc1\n"
  "s_add_u32 s62, s57, 1\n"
  "s_and_b32 s62, s62, 3\n"
  "s_lshl_b32 s62, s62, 11\n"
  "s_add_u32 s62, s62, s92\n"
  "buffer_load_dwordx4 v[144:147], v2, s[12:15], s62 offen offset:0 sc1\n"
  "buffer_load_dwordx4 v[148:151], v2, s[12:15], s62 offen offset:1024 sc1\n"
  "buffer_load_dwordx4 v[152:155], v206, s[12:15], s62 offen offset:0 sc1\n"
  "buffer_load_dwordx4 v[156:159], v206, s[12:15], s62 offen offset:1024 sc1\n"
  "s_waitcnt vmcnt(22)\n"
  "v_permlane32_swap_b32 v192, v194\n"
  "v_permlane32_swap_b32 v193, v195\n"
  "v_permlane32_swap_b32 v196, v198\n"
  "v_permlane32_swap_b32 v197, v199\n"
  "v_permlane32_swap_b32 v200, v202\n"
  "v_permlane32_swap_b32 v201, v203\n"
  "v_lshlrev_b32 v96, 16, v192\n"
  "v_and_b32 v97, 0xffff0000, v192\n"
  "v_lshlrev_b32 v98, 16, v193\n"
  "v_and_b32 v99, 0xffff0000, v193\n"
  "v_lshlrev_b32 v100, 16, v194\n"
  "v_and_b32 v101, 0xffff0000, v194\n"
  "v_lshlrev_b32 v102, 16, v195\n"
  "v_and_b32 v103, 0xffff0000, v195\n"
  "v_lshlrev_b32 v104, 16, v196\n"
  "v_and_b32 v105, 0xffff0000, v196\n"
  "v_lshlrev_b32 v106, 16, v197\n"
  "v_and_b32 v107, 0xffff0000, v197\n"
  "v_lshlrev_b32 v108, 16, v198\n"
  "v_and_b32 v109, 0xffff0000, v198\n"
  "v_lshlrev_b32 v110, 16, v199\n"
  "v_and_b32 v111, 0xffff0000, v199\n"
  "v_lshlrev_b32 v224, 16, v200\n"
  "v_and_b32 v225, 0xffff0000, v200\n"
  "v_lshlrev_b32 v226, 16, v201\n"
  "v_and_b32 v227, 0xffff0000, v201\n"
  "v_lshlrev_b32 v228, 16, v202\n"
  "v_and_b32 v229, 0xffff0000, v202\n"
  "v_lshlrev_b32 v230, 16, v203\n"
  "v_and_b32 v231, 0xffff0000, v203\n"
  "s_xor_b32 s58, s52, 1\n"
  "s_mul_i32 s58, s58, 6144\n"
  "s_add_u32 s58, s58, 100864\n"
  "v_add_u32 v112, s58, v2\n"
  "s_xor_b32 s58, s52, 2\n"
  "s_mul_i32 s58, s58, 6144\n"
  "s_add_u32 s58, s58, 102912\n"
  "v_add_u32 v113, s58, v2\n"
  "s_xor_b32 s58, s52, 3\n"
  "s_mul_i32 s58, s58, 6144\n"
  "s_add_u32 s58, s58, 104960\n"
  "v_add_u32 v114, s58, v2\n"
  "s_mul_i32 s58, s52, 6144\n"
  "s_add_u32 s58, s58, 100864\n"
  "v_add_u32 v115, s58, v2\n"
  "s_add_u32 s62, s57, 2\n"
  "s_and_b32 s62, s62, 3\n"
  "s_lshl_b32 s62, s62, 11\n"
  "s_add_u32 s62, s62, s92\n"
  "buffer_load_dwordx4 v[160:163], v2, s[12:15], s62 offen offset:0 sc1\n"
  "buffer_load_dwordx4 v[164:167], v2, s[12:15], s62 offen offset:1024 sc1\n"
  "buffer_load_dwordx4 v[168:171], v206, s[12:15], s62 offen offset:0 sc1\n"
  "buffer_load_dwordx4 v[172:175], v206, s[12:15], s62 offen offset:1024 sc1\n"
  "s_waitcnt vmcnt(8)\n"
  "v_xor_b32 v5, s100, v131\n"
  "v_bitop3_b32 v5, v135, s100, v5 bitop3:0xbe\n"
  "v_bitop3_b32 v5, v139, s100, v5 bitop3:0xbe\n"
  "v_bitop3_b32 v5, v143, s100, v5 bitop3:0xbe\n"
  "v_cmp_lt_u32 vcc, 0xffff, v5\n"
  "s_cbranch_vccnz .Lb0_f\n"
  ".Lb0_k:\n"
  "v_mfma_f32_32x32x16_bf16 a[240:255], a[164:167], v[128:131], 0\n"
  "v_mfma_f32_32x32x16_bf16 a[224:239], a[160:163], v[128:131], 0\n"
  "v_mfma_f32_32x32x16_bf16 a[240:255], a[172:175], v[132:135], a[240:255]\n"
  "v_mfma_f32_32x32x16_bf16 a[224:239], a[168:171], v[132:135], a[224:239]\n"
  "v_mfma_f32_32x32x16_bf16 v[32:47], a[164:167], v[136:139], 0\n"
  "v_mfma_f32_32x32x16_bf16 v[16:31], a[160:163], v[136:139], 0\n"
  "v_mfma_f32_32x32x16_bf16 v[32:47], a[172:175], v[140:143], v[32:47]\n"
  "v_mfma_f32_32x32x16_bf16 v[16:31], a[168:171], v[140:143], v[16:31]\n"
  "s_add_u32 s62, s57, 3\n"
  "s_and_b32 s62, s62, 3\n"
  "s_lshl_b32 s62, s62, 11\n"
  "s_add_u32 s62, s62, s92\n"
  "buffer_load_dwordx4 v[176:179], v2, s[12:15], s62 offen offset:0 sc1\n"
  "buffer_load_dwordx4 v[180:183], v2, s[12:15], s62 offen offset:1024 sc1\n"
  "buffer_load_dwordx4 v[184:187], v206, s[12:15], s62 offen offset:0 sc1\n"
  "buffer_load_dwordx4 v[188:191], v206, s[12:15], s62 offen offset:1024 sc1\n"
  "s_waitcnt vmcnt(8)\n"
  "v_xor_b32 v5, s100, v147\n"
  "v_bitop3_b32 v5, v151, s100, v5 bitop3:0xbe\n"
  "v_bitop3_b32 v5, v155, s100, v5 bitop3:0xbe\n"
  "v_bitop3_b32 v5, v159, s100, v5 bitop3:0xbe\n"
  "v_cmp_lt_u32 vcc, 0xffff, v5\n"
  "s_cbranch_vccnz .Lb1_f\n"
  ".Lb1_k:\n"
  "v_mfma_f32_32x32x16_bf16 a[240:255], a[180:183], v[144:147], a[240:255]\n"
  "v_mfma_f32_32x32x16_bf16 a[224:239], a[176:179], v[144:147], a[224:239]\n"
  "v_mfma_f32_32x32x16_bf16 a[240:255], a[188:191], v[148:151], a[240:255]\n"
  "v_mfma_f32_32x32x16_bf16 a[224:239], a[184:187], v[148:151], a[224:239]\n"
  "v_mfma_f32_32x32x16_bf16 v[32:47], a[180:183], v[152:155], v[32:47]\n"
  "v_mfma_f32_32x32x16_bf16 v[16:31], a[176:179], v[152:155], v[16:31]\n"
  "v_mfma_f32_32x32x16_bf16 v[32:47], a[188:191], v[156:159], v[32:47]\n"
  "v_mfma_f32_32x32x16_bf16 v[16:31], a[184:187], v[156:159], v[16:31]\n"
  "s_waitcnt vmcnt(4)\n"
  "v_xor_b32 v5, s100, v163\n"
  "v_bitop3_b32 v5, v167, s100, v5 bitop3:0xbe\n"
  "v_bitop3_b32 v5, v171, s100, v5 bitop3:0xbe\n"
  "v_bitop3_b32 v5, v175, s100, v5 bitop3:0xbe\n"
  "v_cmp_lt_u32 vcc, 0xffff, v5\n"
  "s_cbranch_vccnz .Lb2_f\n"
  ".Lb2_k:\n"
  "v_mfma_f32_32x32x16_bf16 a[240:255], a[196:199], v[160:163], a[240:255]\n"
  "s_bitcmp1_b32 0x2a54, s54\n"
  "s_cbranch_scc0 .Lq2\n"
  "s_add_u32 s67, s57, 1\n"
  "s_and_b32 s67, s67, 3\n"
  "s_mul_i32 s67, s67, 14336\n"
  "s_add_u32 s67, s67, s96\n"
  "buffer_load_dwordx4 a[8:11], v12, s[12:15], s67 offen offset:0 sc1\n"
  ".Lq2:\n"
  "v_mfma_f32_32x32x16_bf16 a[224:239], a[192:195], v[160:163], a[224:239]\n"
  "v_mfma_f32_32x32x16_bf16 a[240:255], a[204:207], v[164:167], a[240:255]\n"
  "s_bitcmp1_b32 0x2a54, s54\n"
  "s_cbranch_scc0 .Lq3\n"
  "buffer_load_dwordx4 a[12:15], v13, s[12:15], s67 offen offset:0 sc1\n"
  ".Lq3:\n"
  "v_mfma_f32_32x32x16_bf16 a[224:239], a[200:203], v[164:167], a[224:239]\n"
  "v_mfma_f32_32x32x16_bf16 v[32:47], a[196:199], v[168:171], v[32:47]\n"
  "s_bitcmp1_b32 0x2a54, s54\n"
  "s_cbranch_scc0 .Lq4\n"
  "buffer_load_dwordx4 a[40:43], v12, s[12:15], s67 offen offset:2048 sc1\n"
  ".Lq4:\n"
  "v_mfma_f32_32x32x16_bf16 v[16:31], a[192:195], v[168:171], v[16:31]\n"
  "v_mfma_f32_32x32x16_bf16 v[32:47], a[204:207], v[172:175], v[32:47]\n"
  "s_bitcmp1_b32 0x2a54, s54\n"
  "s_cbranch_scc0 .Lq5\n"
  "buffer_load_dwordx4 a[44:47], v13, s[12:15], s67 offen offset:2048 sc1\n"
  ".Lq5:\n"
  "v_mfma_f32_32x32x16_bf16 v[16:31], a[200:203], v[172:175], v[16:31]\n"
  "s_waitcnt vmcnt(0)\n"
  "v_xor_b32 v5, s100, v179\n"
  "v_bitop3_b32 v5, v183, s100, v5 bitop3:0xbe\n"
  "v_bitop3_b32 v5, v187, s100, v5 bitop3:0xbe\n"
  "v_bitop3_b32 v5, v191, s100, v5 bitop3:0xbe\n"
  "v_cmp_lt_u32 vcc, 0xffff, v5\n"
  "s_cbranch_vccnz .Lb3_f\n"
  ".Lb3_k:\n"
  "v_mfma_f32_32x32x16_bf16 a[240:255], a[212:215], v[176:179], a[240:255]\n"
  "s_bitcmp1_b32 0x2a54, s54\n"
  "s_cbranch_scc0 .Lq6\n"
  "s_add_u32 s67, s57, 2\n"
  "s_and_b32 s67, s67, 3\n"
  "s_mul_i32 s67, s67, 14336\n"
  "s_add_u32 s67, s67, s96\n"
  "buffer_load_dwordx4 a[16:19], v12, s[12:15], s67 offen offset:0 sc1\n"
  ".Lq6:\n"
  "v_mfma_f32_32x32x16_bf16 a[224:239], a[208:211], v[176:179], a[224:239]\n"
  "v_mfma_f32_32x32x16_bf16 a[240:255], a[220:223], v[180:183], a[240:255]\n"
  "s_bitcmp1_b32 0x2a54, s54\n"
  "s_cbranch_scc0 .Lq7\n"
  "buffer_load_dwordx4 a[20:23], v13, s[12:15], s67 offen offset:0 sc1\n"
  ".Lq7:\n"
  "v_mfma_f32_32x32x16_bf16 a[224:239], a[216:219], v[180:183], a[224:239]\n"
  "v_mfma_f32_32x32x16_bf16 v[32:47], a[212:215], v[184:187], v[32:47]\n"
  "s_bitcmp1_b32 0x2a54, s54\n"
  "s_cbranch_scc0 .Lq8\n"
  "buffer_load_dwordx4 a[48:51], v12, s[12:15], s67 offen offset:2048 sc1\n"
  ".Lq8:\n"
  "v_mfma_f32_32x32x16_bf16 v[16:31], a[208:211], v[184:187], v[16:31]\n"
  "v_mfma_f32_32x32x16_bf16 v[32:47], a[220:223], v[188:191], v[32:47]\n"
  "s_bitcmp1_b32 0x2a54, s54\n"
  "s_cbranch_scc0 .Lq9\n"
  "buffer_load_dwordx4 a[52:55], v13, s[12:15], s67 offen offset:2048 sc1\n"
  ".Lq9:\n"
  "v_mfma_f32_32x32x16_bf16 v[16:31], a[216:219], v[188:191], v[16:31]\n"
  "s_nop 0\n"
  "ds_write_b128 v113, a[240:243] offset:0\n"
  "ds_write_b128 v113, a[244:247] offset:1024\n"
  "s_bitcmp1_b32 0x2a54, s54\n"
  "s_cbranch_scc0 .Lq10\n"
  "s_add_u32 s67, s57, 3\n"
  "s_and_b32 s67, s67, 3\n"
  "s_mul_i32 s67, s67, 14336\n"
  "s_add_u32 s67, s67, s96\n"
  "buffer_load_dwordx4 a[24:27], v12, s[12:15], s67 offen offset:0 sc1\n"
  ".Lq10:\n"
  "ds_write_b128 v114, a[248:251] offset:0\n"
  "ds_write_b128 v114, a[252:255] offset:1024\n"
  "s_bitcmp1_b32 0x2a54, s54\n"
  "s_cbranch_scc0 .Lq11\n"
  "buffer_load_dwordx4 a[28:31], v13, s[12:15], s67 offen offset:0 sc1\n"
  ".Lq11:\n"
  "ds_write_b128 v112, a[232:235] offset:0\n"
  "ds_write_b128 v112, a[236:239] offset:1024\n"
  "v_accvgpr_read_b32 v120, a224\n"
  "v_accvgpr_read_b32 v121, a225\n"
  "v_accvgpr_read_b32 v122, a226\n"
  "v_accvgpr_read_b32 v123, a227\n"
  "v_accvgpr_read_b32 v124, a228\n"
  "v_accvgpr_read_b32 v125, a229\n"
  "v_accvgpr_read_b32 v126, a230\n"
  "v_accvgpr_read_b32 v127, a231\n"
  "ds_write_b128 v113, v[32:35] offset:24576\n"
  "ds_write_b128 v113, v[36:39] offset:25600\n"
  "s_bitcmp1_b32 0x2a54, s54\n"
  "s_cbranch_scc0 .Lq12\n"
  "buffer_load_dwordx4 a[56:59], v12, s[12:15], s67 offen offset:2048 sc1\n"
  ".Lq12:\n"
  "ds_write_b128 v114, v[40:43] offset:24576\n"
  "ds_write_b128 v114, v[44:47] offset:25600\n"
  "s_bitcmp1_b32 0x2a54, s54\n"
  "s_cbranch_scc0 .Lq13\n"
  "buffer_load_dwordx4 a[60:63], v13, s[12:15], s67 offen offset:2048 sc1\n"
  ".Lq13:\n"
  "ds_write_b128 v112, v[24:27] offset:24576\n"
  "ds_write_b128 v112, v[28:31] offset:25600\n"
  "v_pk_add_f32 v[96:97], v[96:97], v[120:121]\n"
  "v_pk_add_f32 v[98:99], v[98:99], v[122:123]\n"
  "v_pk_add_f32 v[100:101], v[100:101], v[124:125]\n"
  "v_pk_add_f32 v[102:103], v[102:103], v[126:127]\n"
  "v_pk_add_f32 v[104:105], v[104:105], v[16:17]\n"
  "v_pk_add_f32 v[106:107], v[106:107], v[18:19]\n"
  "v_pk_add_f32 v[108:109], v[108:109], v[20:21]\n"
  "v_pk_add_f32 v[110:111], v[110:111], v[22:23]\n"
  "s_waitcnt lgkmcnt(0)\n"
  "s_barrier\n"
  "ds_read_b128 v[48:51], v115 offset:0\n"
  "ds_read_b128 v[52:55], v115 offset:1024\n"
  "ds_read_b128 v[56:59], v115 offset:2048\n"
  "ds_read_b128 v[60:63], v115 offset:3072\n"
  "ds_read_b128 v[64:67], v115 offset:4096\n"
  "ds_read_b128 v[68:71], v115 offset:5120\n"
  "s_bitcmp1_b32 0x2a54, s54\n"
  "s_cbranch_scc0 .Lq14\n"
  "s_add_u32 s67, s57, 0\n"
  "s_and_b32 s67, s67, 3\n"
  "s_mul_i32 s67, s67, 14336\n"
  "s_add_u32 s67, s67, s96\n"
  "s_add_u32 s67, s67, 4096\n"
  "buffer_load_dwordx4 a[64:67], v12, s[12:15], s67 offen offset:0 sc1\n"
  ".Lq14:\n"
  "s_bitcmp1_b32 0x2a54, s54\n"
  "s_cbranch_scc0 .Lq15\n"
  "buffer_load_dwordx4 a[68:71], v13, s[12:15], s67 offen offset:0 sc1\n"
  ".Lq15:\n"
  "ds_read_b128 v[72:75], v115 offset:24576\n"
  "ds_read_b128 v[76:79], v115 offset:25600\n"
  "ds_read_b128 v[80:83], v115 offset:26624\n"
  "ds_read_b128 v[84:87], v115 offset:27648\n"
  "ds_read_b128 v[88:91], v115 offset:28672\n"
  "ds_read_b128 v[92:95], v115 offset:29696\n"
  "s_bitcmp1_b32 0x2a54, s54\n"
  "s_cbranch_scc0 .Lq16\n"
  "buffer_load_dwordx4 a[96:99], v12, s[12:15], s67 offen offset:2048 sc1\n"
  ".Lq16:\n"
  "s_bitcmp1_b32 0x2a54, s54\n"
  "s_cbranch_scc0 .Lq17\n"
  "buffer_load_dwordx4 a[100:103], v13, s[12:15], s67 offen offset:2048 sc1\n"
  ".Lq17:\n"
  "s_waitcnt lgkmcnt(10)\n"
  "v_pk_add_f32 v[96:97], v[96:97], v[48:49]\n"
  "v_pk_add_f32 v[98:99], v[98:99], v[50:51]\n"
  "v_pk_add_f32 v[100:101], v[100:101], v[52:53]\n"
  "v_pk_add_f32 v[102:103], v[102:103], v[54:55]\n"
  "s_waitcnt lgkmcnt(8)\n"
  "v_pk_add_f32 v[96:97], v[96:97], v[56:57]\n"
  "v_pk_add_f32 v[98:99], v[98:99], v[58:59]\n"
  "v_pk_add_f32 v[100:101], v[100:101], v[60:61]\n"
  "v_pk_add_f32 v[102:103], v[102:103], v[62:63]\n"
  "s_waitcnt lgkmcnt(6)\n"
  "v_pk_add_f32 v[96:97], v[96:97], v[64:65]\n"
  "v_pk_add_f32 v[98:99], v[98:99], v[66:67]\n"
  "v_pk_add_f32 v[100:101], v[100:101], v[68:69]\n"
  "v_pk_add_f32 v[102:103], v[102:103], v[70:71]\n"
  "v_cvt_pk_bf16_f32 v116, v96, v97\n"
  "v_cvt_pk_bf16_f32 v117, v98, v99\n"
  "v_cvt_pk_bf16_f32 v118, v100, v101\n"
  "v_cvt_pk_bf16_f32 v119, v102, v103\n"
  "s_nop 0\n"
  "v_permlane32_swap_b32 v116, v118\n"
  "v_permlane32_swap_b32 v117, v119\n"
  "buffer_store_dwordx4 v[116:119], v2, s[12:15], s80 offen offset:0 sc1\n"
  "s_waitcnt lgkmcnt(4)\n"
  "v_pk_add_f32 v[104:105], v[104:105], v[72:73]\n"
  "v_pk_add_f32 v[106:107], v[106:107], v[74:75]\n"
  "v_pk_add_f32 v[108:109], v[108:109], v[76:77]\n"
  "v_pk_add_f32 v[110:111], v[110:111], v[78:79]\n"
  "s_waitcnt lgkmcnt(2)\n"
  "v_pk_add_f32 v[104:105], v[104:105], v[80:81]\n"
  "v_pk_add_f32 v[106:107], v[106:107], v[82:83]\n"
  "v_pk_add_f32 v[108:109], v[108:109], v[84:85]\n"
  "v_pk_add_f32 v[110:111], v[110:111], v[86:87]\n"
  "s_waitcnt lgkmcnt(0)\n"
  "v_pk_add_f32 v[104:105], v[104:105], v[88:89]\n"
  "v_pk_add_f32 v[106:107], v[106:107], v[90:91]\n"
  "v_pk_add_f32 v[108:109], v[108:109], v[92:93]\n"
  "v_pk_add_f32 v[110:111], v[110:111], v[94:95]\n"
  "v_cvt_pk_bf16_f32 v120, v104, v105\n"
  "v_cvt_pk_bf16_f32 v121, v106, v107\n"
  "v_cvt_pk_bf16_f32 v122, v108, v109\n"
  "v_cvt_pk_bf16_f32 v123, v110, v111\n"
  "s_add_u32 s58, s80, 28672\n"
  "v_permlane32_swap_b32 v120, v122\n"
  "v_permlane32_swap_b32 v121, v123\n"
  "buffer_store_dwordx4 v[120:123], v2, s[12:15], s58 offen offset:0 sc1\n"
  "v_mov_b32 v124, 0\n"
  "v_mov_b32 v125, 0\n"
  "v_mov_b32 v126, 0\n"
  "v_mov_b32 v127, 0\n"
  "v_dot2c_f32_bf16 v124, s70, v116\n"
  "v_dot2c_f32_bf16 v125, v116, v116\n"
  "v_dot2c_f32_bf16 v126, s70, v120\n"
  "v_dot2c_f32_bf16 v127, v120, v120\n"
  "v_dot2c_f32_bf16 v124, s70, v117\n"
  "v_dot2c_f32_bf16 v125, v117, v117\n"
  "v_dot2c_f32_bf16 v126, s70, v121\n"
  "v_dot2c_f32_bf16 v127, v121, v121\n"
  "v_dot2c_f32_bf16 v124, s70, v118\n"
  "v_dot2c_f32_bf16 v125, v118, v118\n"
  "v_dot2c_f32_bf16 v126, s70, v122\n"
  "v_dot2c_f32_bf16 v127, v122, v122\n"
  "v_dot2c_f32_bf16 v124, s70, v119\n"
  "v_dot2c_f32_bf16 v125, v119, v119\n"
  "v_dot2c_f32_bf16 v126, s70, v123\n"
  "v_dot2c_f32_bf16 v127, v123, v123\n"
  "v_pk_add_f32 v[96:97], v[96:97], v[224:225]\n"
  "v_pk_add_f32 v[98:99], v[98:99], v[226:227]\n"
  "v_permlane32_swap_b32 v124, v126\n"
  "v_permlane32_swap_b32 v125, v127\n"
  "v_pk_add_f32 v[124:125], v[124:125], v[126:127]\n"
  "buffer_store_dwordx2 v[124:125], v14, s[12:15], s81 offen sc1\n"
  "v_pk_add_f32 v[100:101], v[100:101], v[228:229]\n"
  "v_pk_add_f32 v[102:103], v[102:103], v[230:231]\n"
  "v_pk_add_f32 v[104:105], v[104:105], v[224:225]\n"
  "v_pk_add_f32 v[106:107], v[106:107], v[226:227]\n"
  "v_pk_add_f32 v[108:109], v[108:109], v[228:229]\n"
  "v_pk_add_f32 v[110:111], v[110:111], v[230:231]\n"
  "v_lshrrev_b32 v5, 5, v1\n"
  "v_bfe_u32 v6, v1, 2, 2\n"
  "v_lshl_add_u32 v5, v5, 2, v6\n"
  "v_mul_u32_u24 v5, 448, v5\n"
  "v_and_b32 v6, 63, v3\n"
  "v_add_u32 v8, v5, v6\n"
  "v_add_u32 v9, 50432, v8\n"
  "s_lshl_b32 s58, s52, 6\n"
  "v_add_u32 v6, s58, v8\n"
  "v_add_u32 v7, s58, v9\n"
  "v_mov_b32 v240, 0\n"
  "v_mov_b32 v241, 0\n"
  "v_mov_b32 v242, 0\n"
  "v_mov_b32 v243, 0\n"
  "ds_read_b64_tr_b16 v[48:49], v6 offset:0\n"
  "ds_read_b64_tr_b16 v[50:51], v6 offset:224\n"
  "ds_read_b64_tr_b16 v[52:53], v6 offset:25216\n"
  "ds_read_b64_tr_b16 v[54:55], v6 offset:25440\n"
  "ds_read_b64_tr_b16 v[56:57], v7 offset:0\n"
  "ds_read_b64_tr_b16 v[58:59], v7 offset:224\n"
  "ds_read_b64_tr_b16 v[60:61], v7 offset:25216\n"
  "ds_read_b64_tr_b16 v[62:63], v7 offset:25440\n"
  "ds_read_b64_tr_b16 v[64:65], v6 offset:3584\n"
  "ds_read_b64_tr_b16 v[66:67], v6 offset:3808\n"
  "ds_read_b64_tr_b16 v[68:69], v6 offset:28800\n"
  "ds_read_b64_tr_b16 v[70:71], v6 offset:29024\n"
  "ds_read_b64_tr_b16 v[72:73], v7 offset:3584\n"
  "ds_read_b64_tr_b16 v[74:75], v7 offset:3808\n"
  "ds_read_b64_tr_b16 v[76:77], v7 offset:28800\n"
  "ds_read_b64_tr_b16 v[78:79], v7 offset:29024\n"
  "s_add_u32 s67, s57, 1\n"
  "s_and_b32 s67, s67, 3\n"
  "s_mul_i32 s67, s67, 14336\n"
  "s_add_u32 s67, s67, s96\n"
  "s_add_u32 s67, s67, 4096\n"
  "buffer_load_dwordx4 a[72:75], v12, s[12:15], s67 offen offset:0 sc1\n"
  "s_waitcnt lgkmcnt(14)\n"
  "v_dot2c_f32_bf16 v240, s70, v48\n"
  "v_dot2c_f32_bf16 v241, s70, v49\n"
  "v_dot2c_f32_bf16 v242, s70, v50\n"
  "v_dot2c_f32_bf16 v243, s70, v51\n"
  "ds_read_b64_tr_b16 v[48:49], v6 offset:7168\n"
  "ds_read_b64_tr_b16 v[50:51], v6 offset:7392\n"
  "buffer_load_dwordx4 a[76:79], v13, s[12:15], s67 offen offset:0 sc1\n"
  "s_waitcnt lgkmcnt(14)\n"
  "v_dot2c_f32_bf16 v240, s70, v52\n"
  "v_dot2c_f32_bf16 v241, s70, v53\n"
  "v_dot2c_f32_bf16 v242, s70, v54\n"
  "v_dot2c_f32_bf16 v243, s70, v55\n"
  "ds_read_b64_tr_b16 v[52:53], v6 offset:32384\n"
  "ds_read_b64_tr_b16 v[54:55], v6 offset:32608\n"
  "buffer_load_dwordx4 a[104:107], v12, s[12:15], s67 offen offset:2048 sc1\n"
  "s_waitcnt lgkmcnt(14)\n"
  "v_dot2c_f32_bf16 v240, s70, v56\n"
  "v_dot2c_f32_bf16 v241, s70, v57\n"
  "v_dot2c_f32_bf16 v242, s70, v58\n"
  "v_dot2c_f32_bf16 v243, s70, v59\n"
  "ds_read_b64_tr_b16 v[56:57], v7 offset:7168\n"
  "ds_read_b64_tr_b16 v[58:59], v7 offset:7392\n"
  "buffer_load_dwordx4 a[108:111], v13, s[12:15], s67 offen offset:2048 sc1\n"
  "s_waitcnt lgkmcnt(14)\n"
  "v_dot2c_f32_bf16 v240, s70, v60\n"
  "v_dot2c_f32_bf16 v241, s70, v61\n"
  "v_dot2c_f32_bf16 v242, s70, v62\n"
  "v_dot2c_f32_bf16 v243, s70, v63\n"
  "ds_read_b64_tr_b16 v[60:61], v7 offset:32384\n"
  "ds_read_b64_tr_b16 v[62:63], v7 offset:32608\n"
  "s_add_u32 s67, s57, 2\n"
  "s_and_b32 s67, s67, 3\n"
  "s_mul_i32 s67, s67, 14336\n"
  "s_add_u32 s67, s67, s96\n"
  "s_add_u32 s67, s67, 4096\n"
  "buffer_load_dwordx4 a[80:83], v12, s[12:15], s67 offen offset:0 sc1\n"
  "s_waitcnt lgkmcnt(14)\n"
  "v_dot2c_f32_bf16 v240, s70, v64\n"
  "v_dot2c_f32_bf16 v241, s70, v65\n"
  "v_dot2c_f32_bf16 v242, s70, v66\n"
  "v_dot2c_f32_bf16 v243, s70, v67\n"
  "ds_read_b64_tr_b16 v[64:65], v6 offset:10752\n"
  "ds_read_b64_tr_b16 v[66:67], v6 offset:10976\n"
  "buffer_load_dwordx4 a[84:87], v13, s[12:15], s67 offen offset:0 sc1\n"
  "s_waitcnt lgkmcnt(14)\n"
  "v_dot2c_f32_bf16 v240, s70, v68\n"
  "v_dot2c_f32_bf16 v241, s70, v69\n"
  "v_dot2c_f32_bf16 v242, s70, v70\n"
  "v_dot2c_f32_bf16 v243, s70, v71\n"
  "ds_read_b64_tr_b16 v[68:69], v6 offset:35968\n"
  "ds_read_b64_tr_b16 v[70:71], v6 offset:36192\n"
  "buffer_load_dwordx4 a[112:115], v12, s[12:15], s67 offen offset:2048 sc1\n"
  "s_waitcnt lgkmcnt(14)\n"
  "v_dot2c_f32_bf16 v240, s70, v72\n"
  "v_dot2c_f32_bf16 v241, s70, v73\n"
  "v_dot2c_f32_bf16 v242, s70, v74\n"
  "v_dot2c_f32_bf16 v243, s70, v75\n"
  "ds_read_b64_tr_b16 v[72:73], v7 offset:10752\n"
  "ds_read_b64_tr_b16 v[74:75], v7 offset:10976\n"
  "buffer_load_dwordx4 a[116:119], v13, s[12:15], s67 offen offset:2048 sc1\n"
  "s_waitcnt lgkmcnt(14)\n"
  "v_dot2c_f32_bf16 v240, s70, v76\n"
  "v_dot2c_f32_bf16 v241, s70, v77\n"
  "v_dot2c_f32_bf16 v242, s70, v78\n"
  "v_dot2c_f32_bf16 v243, s70, v79\n"
  "v_lshlrev_b32 v94, 5, v1\n"
  "s_mov_b32 s63, 6144\n"
  "buffer_load_dwordx4 v[208:211], v94, s[12:15], s63 offen offset:0 sc1\n"
  "buffer_load_dwordx4 v[212:215], v94, s[12:15], s63 offen offset:16 sc1\n"
  "ds_read_b64_tr_b16 v[76:77], v7 offset:35968\n"
  "ds_read_b64_tr_b16 v[78:79], v7 offset:36192\n"
  "s_add_u32 s67, s57, 3\n"
  "s_and_b32 s67, s67, 3\n"
  "s_mul_i32 s67, s67, 14336\n"
  "s_add_u32 s67, s67, s96\n"
  "s_add_u32 s67, s67, 4096\n"
  "buffer_load_dwordx4 a[88:91], v12, s[12:15], s67 offen offset:0 sc1\n"
  "s_waitcnt lgkmcnt(14)\n"
  "v_dot2c_f32_bf16 v240, s70, v48\n"
  "v_dot2c_f32_bf16 v241, s70, v49\n"
  "v_dot2c_f32_bf16 v242, s70, v50\n"
  "v_dot2c_f32_bf16 v243, s70, v51\n"
  "ds_read_b64_tr_b16 v[48:49], v6 offset:14336\n"
  "ds_read_b64_tr_b16 v[50:51], v6 offset:14560\n"
  "buffer_load_dwordx4 a[92:95], v13, s[12:15], s67 offen offset:0 sc1\n"
  "s_waitcnt lgkmcnt(14)\n"
  "v_dot2c_f32_bf16 v240, s70, v52\n"
  "v_dot2c_f32_bf16 v241, s70, v53\n"
  "v_dot2c_f32_bf16 v242, s70, v54\n"
  "v_dot2c_f32_bf16 v243, s70, v55\n"
  "ds_read_b64_tr_b16 v[52:53], v6 offset:39552\n"
  "ds_read_b64_tr_b16 v[54:55], v6 offset:39776\n"
  "buffer_load_dwordx4 a[120:123], v12, s[12:15], s67 offen offset:2048 sc1\n"
  "s_waitcnt lgkmcnt(14)\n"
  "v_dot2c_f32_bf16 v240, s70, v56\n"
  "v_dot2c_f32_bf16 v241, s70, v57\n"
  "v_dot2c_f32_bf16 v242, s70, v58\n"
  "v_dot2c_f32_bf16 v243, s70, v59\n"
  "ds_read_b64_tr_b16 v[56:57], v7 offset:14336\n"
  "ds_read_b64_tr_b16 v[58:59], v7 offset:14560\n"
  "buffer_load_dwordx4 a[124:127], v13, s[12:15], s67 offen offset:2048 sc1\n"
  "s_waitcnt lgkmcnt(14)\n"
  "v_dot2c_f32_bf16 v240, s70, v60\n"
  "v_dot2c_f32_bf16 v241, s70, v61\n"
  "v_dot2c_f32_bf16 v242, s70, v62\n"
  "v_dot2c_f32_bf16 v243, s70, v63\n"
  "ds_read_b64_tr_b16 v[60:61], v7 offset:39552\n"
  "ds_read_b64_tr_b16 v[62:63], v7 offset:39776\n"
  "s_add_u32 s67, s57, 0\n"
  "s_and_b32 s67, s67, 3\n"
  "s_mul_i32 s67, s67, 14336\n"
  "s_add_u32 s67, s67, s96\n"
  "s_add_u32 s67, s67, 8192\n"
  "buffer_load_dwordx4 a[128:131], v12, s[12:15], s67 offen offset:0 sc1\n"
  "s_waitcnt lgkmcnt(14)\n"
  "v_dot2c_f32_bf16 v240, s70, v64\n"
  "v_dot2c_f32_bf16 v241, s70, v65\n"
  "v_dot2c_f32_bf16 v242, s70, v66\n"
  "v_dot2c_f32_bf16 v243, s70, v67\n"
  "ds_read_b64_tr_b16 v[64:65], v6 offset:17920\n"
  "ds_read_b64_tr_b16 v[66:67], v6 offset:18144\n"
  "buffer_load_dwordx4 a[132:135], v13, s[12:15], s67 offen offset:0 sc1\n"
  "s_waitcnt lgkmcnt(14)\n"
  "v_dot2c_f32_bf16 v240, s70, v68\n"
  "v_dot2c_f32_bf16 v241, s70, v69\n"
  "v_dot2c_f32_bf16 v242, s70, v70\n"
  "v_dot2c_f32_bf16 v243, s70, v71\n"
  "ds_read_b64_tr_b16 v[68:69], v6 offset:43136\n"
  "ds_read_b64_tr_b16 v[70:71], v6 offset:43360\n"
  "buffer_load_dwordx4 a[160:163], v12, s[12:15], s67 offen offset:2048 sc1\n"
  "s_waitcnt lgkmcnt(14)\n"
  "v_dot2c_f32_bf16 v240, s70, v72\n"
  "v_dot2c_f32_bf16 v241, s70, v73\n"
  "v_dot2c_f32_bf16 v242, s70, v74\n"
  "v_dot2c_f32_bf16 v243, s70, v75\n"
  "ds_read_b64_tr_b16 v[72:73], v7 offset:17920\n"
  "ds_read_b64_tr_b16 v[74:75], v7 offset:18144\n"
  "buffer_load_dwordx4 a[164:167], v13, s[12:15], s67 offen offset:2048 sc1\n"
  "s_waitcnt lgkmcnt(14)\n"
  "v_dot2c_f32_bf16 v240, s70, v76\n"
  "v_dot2c_f32_bf16 v241, s70, v77\n"
  "v_dot2c_f32_bf16 v242, s70, v78\n"
  "v_dot2c_f32_bf16 v243, s70, v79\n"
  "ds_read_b64_tr_b16 v[76:77], v7 offset:43136\n"
  "ds_read_b64_tr_b16 v[78:79], v7 offset:43360\n"
  "s_add_u32 s67, s57, 1\n"
  "s_and_b32 s67, s67, 3\n"
  "s_mul_i32 s67, s67, 14336\n"
  "s_add_u32 s67, s67, s96\n"
  "s_add_u32 s67, s67, 8192\n"
  "buffer_load_dwordx4 a[136:139], v12, s[12:15], s67 offen offset:0 sc1\n"
  "s_waitcnt lgkmcnt(14)\n"
  "v_dot2c_f32_bf16 v240, s70, v48\n"
  "v_dot2c_f32_bf16 v241, s70, v49\n"
  "v_dot2c_f32_bf16 v242, s70, v50\n"
  "v_dot2c_f32_bf16 v243, s70, v51\n"
  "ds_read_b64_tr_b16 v[48:49], v6 offset:21504\n"
  "ds_read_b64_tr_b16 v[50:51], v6 offset:21728\n"
  "buffer_load_dwordx4 a[140:143], v13, s[12:15], s67 offen offset:0 sc1\n"
  "s_waitcnt lgkmcnt(14)\n"
  "v_dot2c_f32_bf16 v240, s70, v52\n"
  "v_dot2c_f32_bf16 v241, s70, v53\n"
  "v_dot2c_f32_bf16 v242, s70, v54\n"
  "v_dot2c_f32_bf16 v243, s70, v55\n"
  "ds_read_b64_tr_b16 v[52:53], v6 offset:46720\n"
  "ds_read_b64_tr_b16 v[54:55], v6 offset:46944\n"
  "buffer_load_dwordx4 a[168:171], v12, s[12:15], s67 offen offset:2048 sc1\n"
  "s_waitcnt lgkmcnt(14)\n"
  "v_dot2c_f32_bf16 v240, s70, v56\n"
  "v_dot2c_f32_bf16 v241, s70, v57\n"
  "v_dot2c_f32_bf16 v242, s70, v58\n"
  "v_dot2c_f32_bf16 v243, s70, v59\n"
  "ds_read_b64_tr_b16 v[56:57], v7 offset:21504\n"
  "ds_read_b64_tr_b16 v[58:59], v7 offset:21728\n"
  "buffer_load_dwordx4 a[172:175], v13, s[12:15], s67 offen offset:2048 sc1\n"
  "s_waitcnt lgkmcnt(14)\n"
  "v_dot2c_f32_bf16 v240, s70, v60\n"
  "v_dot2c_f32_bf16 v241, s70, v61\n"
  "v_dot2c_f32_bf16 v242, s70, v62\n"
  "v_dot2c_f32_bf16 v243, s70, v63\n"
  "ds_read_b64_tr_b16 v[60:61], v7 offset:46720\n"
  "ds_read_b64_tr_b16 v[62:63], v7 offset:46944\n"
  "s_add_u32 s67, s57, 2\n"
  "s_and_b32 s67, s67, 3\n"
  "s_mul_i32 s67, s67, 14336\n"
  "s_add_u32 s67, s67, s96\n"
  "s_add_u32 s67, s67, 8192\n"
  "buffer_load_dwordx4 a[144:147], v12, s[12:15], s67 offen offset:0 sc1\n"
  "s_waitcnt lgkmcnt(14)\n"
  "v_dot2c_f32_bf16 v240, s70, v64\n"
  "v_dot2c_f32_bf16 v241, s70, v65\n"
  "v_dot2c_f32_bf16 v242, s70, v66\n"
  "v_dot2c_f32_bf16 v243, s70, v67\n"
  "buffer_load_dwordx4 a[148:151], v13, s[12:15], s67 offen offset:0 sc1\n"
  "s_waitcnt lgkmcnt(12)\n"
  "v_dot2c_f32_bf16 v240, s70, v68\n"
  "v_dot2c_f32_bf16 v241, s70, v69\n"
  "v_dot2c_f32_bf16 v242, s70, v70\n"
  "v_dot2c_f32_bf16 v243, s70, v71\n"
  "buffer_load_dwordx4 a[176:179], v12, s[12:15], s67 offen offset:2048 sc1\n"
  "s_waitcnt lgkmcnt(10)\n"
  "v_dot2c_f32_bf16 v240, s70, v72\n"
  "v_dot2c_f32_bf16 v241, s70, v73\n"
  "v_dot2c_f32_bf16 v242, s70, v74\n"
  "v_dot2c_f32_bf16 v243, s70, v75\n"
  "buffer_load_dwordx4 a[180:183], v13, s[12:15], s67 offen offset:2048 sc1\n"
  "s_waitcnt lgkmcnt(8)\n"
  "v_dot2c_f32_bf16 v240, s70, v76\n"
  "v_dot2c_f32_bf16 v241, s70, v77\n"
  "v_dot2c_f32_bf16 v242, s70, v78\n"
  "v_dot2c_f32_bf16 v243, s70, v79\n"
  "s_add_u32 s67, s57, 3\n"
  "s_and_b32 s67, s67, 3\n"
  "s_mul_i32 s67, s67, 14336\n"
  "s_add_u32 s67, s67, s96\n"
  "s_add_u32 s67, s67, 8192\n"
  "buffer_load_dwordx4 a[152:155], v12, s[12:15], s67 offen offset:0 sc1\n"
  "s_waitcnt lgkmcnt(6)\n"
  "v_dot2c_f32_bf16 v240, s70, v48\n"
  "v_dot2c_f32_bf16 v241, s70, v49\n"
  "v_dot2c_f32_bf16 v242, s70, v50\n"
  "v_dot2c_f32_bf16 v243, s70, v51\n"
  "buffer_load_dwordx4 a[156:159], v13, s[12:15], s67 offen offset:0 sc1\n"
  "s_waitcnt lgkmcnt(4)\n"
  "v_dot2c_f32_bf16 v240, s70, v52\n"
  "v_dot2c_f32_bf16 v241, s70, v53\n"
  "v_dot2c_f32_bf16 v242, s70, v54\n"
  "v_dot2c_f32_bf16 v243, s70, v55\n"
  "buffer_load_dwordx4 a[184:187], v12, s[12:15], s67 offen offset:2048 sc1\n"
  "s_waitcnt lgkmcnt(2)\n"
  "v_dot2c_f32_bf16 v240, s70, v56\n"
  "v_dot2c_f32_bf16 v241, s70, v57\n"
  "v_dot2c_f32_bf16 v242, s70, v58\n"
  "v_dot2c_f32_bf16 v243, s70, v59\n"
  "buffer_load_dwordx4 a[188:191], v13, s[12:15], s67 offen offset:2048 sc1\n"
  "s_waitcnt lgkmcnt(0)\n"
  "v_dot2c_f32_bf16 v240, s70, v60\n"
  "v_dot2c_f32_bf16 v241, s70, v61\n"
  "v_dot2c_f32_bf16 v242, s70, v62\n"
  "v_dot2c_f32_bf16 v243, s70, v63\n"
  "s_add_u32 s67, s57, 0\n"
  "s_and_b32 s67, s67, 3\n"
  "s_mul_i32 s67, s67, 14336\n"
  "s_add_u32 s67, s67, s96\n"
  "s_add_u32 s67, s67, 12288\n"
  "buffer_load_dwordx4 a[192:195], v12, s[12:15], s67 offen offset:0 sc1\n"
  "buffer_load_dwordx4 a[196:199], v13, s[12:15], s67 offen offset:0 sc1\n"
  "s_add_u32 s67, s57, 1\n"
  "s_and_b32 s67, s67, 3\n"
  "s_mul_i32 s67, s67, 14336\n"
  "s_add_u32 s67, s67, s96\n"
  "s_add_u32 s67, s67, 12288\n"
  "buffer_load_dwordx4 a[200:203], v12, s[12:15], s67 offen offset:0 sc1\n"
  "buffer_load_dwordx4 a[204:207], v13, s[12:15], s67 offen offset:0 sc1\n"
  "s_lshl_b32 s58, s52, 10\n"
  "s_add_u32 s58, s58, 154112\n"
  "v_add_u32 v6, s58, v2\n"
  "v_mov_b32 v247, 0\n"
  "v_add_f32 v240, v240, v241\n"
  "v_add_f32 v242, v242, v243\n"
  "v_add_f32 v240, v240, v242\n"
  "v_cvt_pk_bf16_f32 v5, v240, v240\n"
  "v_and_b32 v5, 0xffff0000, v5\n"
  "v_sub_f32 v5, v240, v5\n"
  "v_cvt_pk_bf16_f32 v244, v240, v5\n"
  "v_lshl_or_b32 v246, v204, 16, v204\n"
  "v_mov_b32 v245, v244\n"
  "v_cndmask_b32 v246, v246, 0, s[74:75]\n"
  "ds_write_b128 v6, v[244:247]\n"
  "s_lshl_b32 s58, s53, 2\n"
  "s_add_u32 s58, s58, s52\n"
  "s_mul_i32 s59, s58, 28672\n"
  "s_add_u32 s92, s59, 5677056\n"
  "s_mul_i32 s58, s58, 7168\n"
  "s_add_u32 s91, s58, 8192\n"
  "v_lshrrev_b32 v5, 5, v1\n"
  "v_mul_u32_u24 v5, 3584, v5\n"
  "v_sub_u32 v14, v14, v5\n"
  "v_mov_b32 v15, v205\n"
  "s_waitcnt lgkmcnt(0)\n"
  ".Lf1:\n"
  "s_waitcnt vmcnt(24)\n"
  "v_cmp_ne_u64 s[58:59], s[44:45], v[208:209]\n"
  "v_cmp_ne_u64 s[60:61], s[44:45], v[210:211]\n"
  "v_cmp_ne_u64 vcc, s[44:45], v[212:213]\n"
  "s_or_b64 s[58:59], s[58:59], s[60:61]\n"
  "v_cmp_ne_u64 s[60:61], s[44:45], v[214:215]\n"
  "s_or_b64 vcc, vcc, s[58:59]\n"
  "s_or_b64 vcc, vcc, s[60:61]\n"
  "s_and_b64 vcc, vcc, s[48:49]\n"
  "s_cbranch_scc0 .Lf1_ok\n"
  "s_mov_b32 s77, 6144\n"
  "buffer_load_dwordx4 v[208:211], v94, s[12:15], s77 offen offset:0 sc1\n"
  "buffer_load_dwordx4 v[212:215], v94, s[12:15], s77 offen offset:16 sc1\n"
  "s_sub_u32 s76, s76, 1\n"
  "s_cselect_b64 s[48:49], 0, s[48:49]\n"
  "s_waitcnt vmcnt(0)\n"
  "s_branch .Lf1\n"
  ".Lf1_ok:\n"
  "s_add_u32 s62, s55, 0\n"
  "s_sub_u32 s64, s62, 7\n"
  "s_min_u32 s62, s62, s64\n"
  "s_lshl_b32 s62, s62, 12\n"
  "s_add_u32 s62, s62, s92\n"
  "buffer_load_dwordx4 v[128:131], v2, s[12:15], s62 offen offset:0 sc1\n"
  "buffer_load_dwordx4 v[132:135], v2, s[12:15], s62 offen offset:1024 sc1\n"
  "buffer_load_dwordx4 v[136:139], v2, s[12:15], s62 offen offset:2048 sc1\n"
  "buffer_load_dwordx4 v[140:143], v2, s[12:15], s62 offen offset:3072 sc1\n"
  "s_add_u32 s62, s55, 1\n"
  "s_sub_u32 s64, s62, 7\n"
  "s_min_u32 s62, s62, s64\n"
  "s_lshl_b32 s62, s62, 12\n"
  "s_add_u32 s62, s62, s92\n"
  "buffer_load_dwordx4 v[144:147], v2, s[12:15], s62 offen offset:0 sc1\n"
  "buffer_load_dwordx4 v[148:151], v2, s[12:15], s62 offen offset:1024 sc1\n"
  "buffer_load_dwordx4 v[152:155], v2, s[12:15], s62 offen offset:2048 sc1\n"
  "buffer_load_dwordx4 v[156:159], v2, s[12:15], s62 offen offset:3072 sc1\n"
  "s_add_u32 s62, s55, 2\n"
  "s_sub_u32 s64, s62, 7\n"
  "s_min_u32 s62, s62, s64\n"
  "s_lshl_b32 s62, s62, 12\n"
  "s_add_u32 s62, s62, s92\n"
  "buffer_load_dwordx4 v[160:163], v2, s[12:15], s62 offen offset:0 sc1\n"
  "buffer_load_dwordx4 v[164:167], v2, s[12:15], s62 offen offset:1024 sc1\n"
  "buffer_load_dwordx4 v[168:171], v2, s[12:15], s62 offen offset:2048 sc1\n"
  "buffer_load_dwordx4 v[172:175], v2, s[12:15], s62 offen offset:3072 sc1\n"
  "v_add_u32 v6, 154112, v2\n"
  "v_add_u32 v11, 114688, v2\n"
  "s_barrier\n"
  "ds_read_b128 v[112:115], v6 offset:0\n"
  "ds_read_b128 v[116:119], v6 offset:1024\n"
  "ds_read_b128 v[120:123], v6 offset:2048\n"
  "ds_read_b128 v[124:127], v6 offset:3072\n"
  "ds_read_b64_tr_b16 v[64:65], v8 offset:0\n"
  "ds_read_b64_tr_b16 v[66:67], v8 offset:224\n"
  "ds_read_b64_tr_b16 v[68:69], v8 offset:64\n"
  "ds_read_b64_tr_b16 v[70:71], v8 offset:288\n"
  "ds_read_b64_tr_b16 v[72:73], v8 offset:25216\n"
  "ds_read_b64_tr_b16 v[74:75], v8 offset:25440\n"
  "ds_read_b64_tr_b16 v[76:77], v8 offset:25280\n"
  "ds_read_b64_tr_b16 v[78:79], v8 offset:25504\n"
  "ds_read_b64_tr_b16 v[80:81], v9 offset:0\n"
  "ds_read_b64_tr_b16 v[82:83], v9 offset:224\n"
  "ds_read_b64_tr_b16 v[84:85], v9 offset:64\n"
  "ds_read_b64_tr_b16 v[86:87], v9 offset:288\n"
  "ds_read_b64_tr_b16 v[88:89], v9 offset:25216\n"
  "ds_read_b64_tr_b16 v[90:91], v9 offset:25440\n"
  "s_waitcnt vmcnt(8)\n"
  "v_max3_u32 v5, v128, v130, v132\n"
  "v_max3_u32 v5, v5, v134, v136\n"
  "v_max3_u32 v5, v5, v138, v140\n"
  "v_max3_u32 v5, v5, v142, v142\n"
  "v_cmp_eq_u32 vcc, -1, v5\n"
  "s_cbranch_vccnz .Lc0_f\n"
  ".Lc0_k:\n"
  "ds_read_b64_tr_b16 v[92:93], v9 offset:25280\n"
  "ds_read_b64_tr_b16 v[94:95], v9 offset:25504\n"
  "s_waitcnt lgkmcnt(12)\n"
  "v_mfma_f32_32x32x16_bf16 v[16:31], v[64:67], v[128:131], 0\n"
  "s_add_u32 s62, s55, 3\n"
  "s_sub_u32 s64, s62, 7\n"
  "s_min_u32 s62, s62, s64\n"
  "s_lshl_b32 s62, s62, 12\n"
  "s_add_u32 s62, s62, s92\n"
  "buffer_load_dwordx4 v[176:179], v2, s[12:15], s62 offen offset:0 sc1\n"
  "ds_read_b64_tr_b16 v[64:65], v8 offset:3584\n"
  "ds_read_b64_tr_b16 v[66:67], v8 offset:3808\n"
  "v_mfma_f32_32x32x16_bf16 v[32:47], v[68:71], v[128:131], 0\n"
  "s_waitcnt vmcnt(5)\n"
  "v_max3_u32 v5, v144, v146, v148\n"
  "ds_read_b64_tr_b16 v[68:69], v8 offset:3648\n"
  "ds_read_b64_tr_b16 v[70:71], v8 offset:3872\n"
  "s_waitcnt lgkmcnt(12)\n"
  "v_mfma_f32_32x32x16_bf16 v[16:31], v[72:75], v[132:135], v[16:31]\n"
  "buffer_load_dwordx4 v[180:183], v2, s[12:15], s62 offen offset:1024 sc1\n"
  "ds_read_b64_tr_b16 v[72:73], v8 offset:28800\n"
  "ds_read_b64_tr_b16 v[74:75], v8 offset:29024\n"
  "v_mfma_f32_32x32x16_bf16 v[32:47], v[76:79], v[132:135], v[32:47]\n"
  "v_max3_u32 v5, v5, v150, v152\n"
  "v_max3_u32 v5, v5, v154, v156\n"
  "ds_read_b64_tr_b16 v[76:77], v8 offset:28864\n"
  "ds_read_b64_tr_b16 v[78:79], v8 offset:29088\n"
  "s_waitcnt lgkmcnt(12)\n"
  "v_mfma_f32_32x32x16_bf16 v[16:31], v[80:83], v[136:139], v[16:31]\n"
  "buffer_load_dwordx4 v[184:187], v2, s[12:15], s62 offen offset:2048 sc1\n"
  "ds_read_b64_tr_b16 v[80:81], v9 offset:3584\n"
  "ds_read_b64_tr_b16 v[82:83], v9 offset:3808\n"
  "v_mfma_f32_32x32x16_bf16 v[32:47], v[84:87], v[136:139], v[32:47]\n"
  "v_max3_u32 v5, v5, v158, v158\n"
  "v_cmp_eq_u32 vcc, -1, v5\n"
  "ds_read_b64_tr_b16 v[84:85], v9 offset:3648\n"
  "ds_read_b64_tr_b16 v[86:87], v9 offset:3872\n"
  "s_waitcnt lgkmcnt(12)\n"
  "v_mfma_f32_32x32x16_bf16 v[16:31], v[88:91], v[140:143], v[16:31]\n"
  "buffer_load_dwordx4 v[188:191], v2, s[12:15], s62 offen offset:3072 sc1\n"
  "ds_read_b64_tr_b16 v[88:89], v9 offset:28800\n"
  "ds_read_b64_tr_b16 v[90:91], v9 offset:29024\n"
  "v_mfma_f32_32x32x16_bf16 v[32:47], v[92:95], v[140:143], v[32:47]\n"
  "s_cbranch_vccnz .Lc1_f\n"
  ".Lc1_k:\n"
  "ds_read_b64_tr_b16 v[92:93], v9 offset:28864\n"
  "ds_read_b64_tr_b16 v[94:95], v9 offset:29088\n"
  "s_waitcnt lgkmcnt(12)\n"
  "v_mfma_f32_32x32x16_bf16 v[16:31], v[64:67], v[144:147], v[16:31]\n"
  "s_add_u32 s62, s55, 4\n"
  "s_sub_u32 s64, s62, 7\n"
  "s_min_u32 s62, s62, s64\n"
  "s_lshl_b32 s62, s62, 12\n"
  "s_add_u32 s62, s62, s92\n"
  "buffer_load_dwordx4 v[192:195], v2, s[12:15], s62 offen offset:0 sc1\n"
  "ds_read_b64_tr_b16 v[64:65], v8 offset:7168\n"
  "ds_read_b64_tr_b16 v[66:67], v8 offset:7392\n"
  "v_mfma_f32_32x32x16_bf16 v[32:47], v[68:71], v[144:147], v[32:47]\n"
  "s_waitcnt vmcnt(5)\n"
  "v_max3_u32 v5, v160, v162, v164\n"
  "ds_read_b64_tr_b16 v[68:69], v8 offset:7232\n"
  "ds_read_b64_tr_b16 v[70:71], v8 offset:7456\n"
  "s_waitcnt lgkmcnt(12)\n"
  "v_mfma_f32_32x32x16_bf16 v[16:31], v[72:75], v[148:151], v[16:31]\n"
  "buffer_load_dwordx4 v[196:199], v2, s[12:15], s62 offen offset:1024 sc1\n"
  "ds_read_b64_tr_b16 v[72:73], v8 offset:32384\n"
  "ds_read_b64_tr_b16 v[74:75], v8 offset:32608\n"
  "v_mfma_f32_32x32x16_bf16 v[32:47], v[76:79], v[148:151], v[32:47]\n"
  "v_max3_u32 v5, v5, v166, v168\n"
  "v_max3_u32 v5, v5, v170, v172\n"
  "ds_read_b64_tr_b16 v[76:77], v8 offset:32448\n"
  "ds_read_b64_tr_b16 v[78:79], v8 offset:32672\n"
  "s_waitcnt lgkmcnt(12)\n"
  "v_mfma_f32_32x32x16_bf16 v[16:31], v[80:83], v[152:155], v[16:31]\n"
  "buffer_load_dwordx4 v[200:203], v2, s[12:15], s62 offen offset:2048 sc1\n"
  "ds_read_b64_tr_b16 v[80:81], v9 offset:7168\n"
  "ds_read_b64_tr_b16 v[82:83], v9 offset:7392\n"
  "v_mfma_f32_32x32x16_bf16 v[32:47], v[84:87], v[152:155], v[32:47]\n"
  "v_max3_u32 v5, v5, v174, v174\n"
  "v_cmp_eq_u32 vcc, -1, v5\n"
  "ds_read_b64_tr_b16 v[84:85], v9 offset:7232\n"
  "ds_read_b64_tr_b16 v[86:87], v9 offset:7456\n"
  "s_waitcnt lgkmcnt(12)\n"
  "v_mfma_f32_32x32x16_bf16 v[16:31], v[88:91], v[156:159], v[16:31]\n"
  "buffer_load_dwordx4 v[204:207], v2, s[12:15], s62 offen offset:3072 sc1\n"
  "ds_read_b64_tr_b16 v[88:89], v9 offset:32384\n"
  "ds_read_b64_tr_b16 v[90:91], v9 offset:32608\n"
  "v_mfma_f32_32x32x16_bf16 v[32:47], v[92:95], v[156:159], v[32:47]\n"
  "s_cbranch_vccnz .Lc2_f\n"
  ".Lc2_k:\n"
  "ds_read_b64_tr_b16 v[92:93], v9 offset:32448\n"
  "ds_read_b64_tr_b16 v[94:95], v9 offset:32672\n"
  "s_waitcnt lgkmcnt(12)\n"
  "v_mfma_f32_32x32x16_bf16 v[16:31], v[64:67], v[160:163], v[16:31]\n"
  "s_add_u32 s62, s55, 5\n"
  "s_sub_u32 s64, s62, 7\n"
  "s_min_u32 s62, s62, s64\n"
  "s_lshl_b32 s62, s62, 12\n"
  "s_add_u32 s62, s62, s92\n"
  "buffer_load_dwordx4 v[208:211], v2, s[12:15], s62 offen offset:0 sc1\n"
  "ds_read_b64_tr_b16 v[64:65], v8 offset:10752\n"
  "ds_read_b64_tr_b16 v[66:67], v8 offset:10976\n"
  "v_mfma_f32_32x32x16_bf16 v[32:47], v[68:71], v[160:163], v[32:47]\n"
  "s_waitcnt vmcnt(5)\n"
  "v_max3_u32 v5, v176, v178, v180\n"
  "ds_read_b64_tr_b16 v[68:69], v8 offset:10816\n"
  "ds_read_b64_tr_b16 v[70:71], v8 offset:11040\n"
  "s_waitcnt lgkmcnt(12)\n"
  "v_mfma_f32_32x32x16_bf16 v[16:31], v[72:75], v[164:167], v[16:31]\n"
  "buffer_load_dwordx4 v[212:215], v2, s[12:15], s62 offen offset:1024 sc1\n"
  "ds_read_b64_tr_b16 v[72:73], v8 offset:35968\n"
  "ds_read_b64_tr_b16 v[74:75], v8 offset:36192\n"
  "v_mfma_f32_32x32x16_bf16 v[32:47], v[76:79], v[164:167], v[32:47]\n"
  "v_max3_u32 v5, v5, v182, v184\n"
  "v_max3_u32 v5, v5, v186, v188\n"
  "ds_read_b64_tr_b16 v[76:77], v8 offset:36032\n"
  "ds_read_b64_tr_b16 v[78:79], v8 offset:36256\n"
  "s_waitcnt lgkmcnt(12)\n"
  "v_mfma_f32_32x32x16_bf16 v[16:31], v[80:83], v[168:171], v[16:31]\n"
  "buffer_load_dwordx4 v[216:219], v2, s[12:15], s62 offen offset:2048 sc1\n"
  "ds_read_b64_tr_b16 v[80:81], v9 offset:10752\n"
  "ds_read_b64_tr_b16 v[82:83], v9 offset:10976\n"
  "v_mfma_f32_32x32x16_bf16 v[32:47], v[84:87], v[168:171], v[32:47]\n"
  "v_max3_u32 v5, v5, v190, v190\n"
  "v_cmp_eq_u32 vcc, -1, v5\n"
  "ds_read_b64_tr_b16 v[84:85], v9 offset:10816\n"
  "ds_read_b64_tr_b16 v[86:87], v9 offset:11040\n"
  "s_waitcnt lgkmcnt(12)\n"
  "v_mfma_f32_32x32x16_bf16 v[16:31], v[88:91], v[172:175], v[16:31]\n"
  "buffer_load_dwordx4 v[220:223], v2, s[12:15], s62 offen offset:3072 sc1\n"
  "ds_read_b64_tr_b16 v[88:89], v9 offset:35968\n"
  "ds_read_b64_tr_b16 v[90:91], v9 offset:36192\n"
  "v_mfma_f32_32x32x16_bf16 v[32:47], v[92:95], v[172:175], v[32:47]\n"
  "s_cbranch_vccnz .Lc3_f\n"
  ".Lc3_k:\n"
  "ds_read_b64_tr_b16 v[92:93], v9 offset:36032\n"
  "ds_read_b64_tr_b16 v[94:95], v9 offset:36256\n"
  "s_waitcnt lgkmcnt(12)\n"
  "v_mfma_f32_32x32x16_bf16 v[16:31], v[64:67], v[176:179], v[16:31]\n"
  "s_add_u32 s62, s55, 6\n"
  "s_sub_u32 s64, s62, 7\n"
  "s_min_u32 s62, s62, s64\n"
  "s_lshl_b32 s62, s62, 12\n"
  "s_add_u32 s62, s62, s92\n"
  "buffer_load_dwordx4 v[224:227], v2, s[12:15], s62 offen offset:0 sc1\n"
  "ds_read_b64_tr_b16 v[64:65], v8 offset:14336\n"
  "ds_read_b64_tr_b16 v[66:67], v8 offset:14560\n"
  "v_mfma_f32_32x32x16_bf16 v[32:47], v[68:71], v[176:179], v[32:47]\n"
  "s_waitcnt vmcnt(5)\n"
  "v_max3_u32 v5, v192, v194, v196\n"
  "ds_read_b64_tr_b16 v[68:69], v8 offset:14400\n"
  "ds_read_b64_tr_b16 v[70:71], v8 offset:14624\n"
  "s_waitcnt lgkmcnt(12)\n"
  "v_mfma_f32_32x32x16_bf16 v[16:31], v[72:75], v[180:183], v[16:31]\n"
  "buffer_load_dwordx4 v[228:231], v2, s[12:15], s62 offen offset:1024 sc1\n"
  "ds_read_b64_tr_b16 v[72:73], v8 offset:39552\n"
  "ds_read_b64_tr_b16 v[74:75], v8 offset:39776\n"
  "v_mfma_f32_32x32x16_bf16 v[32:47], v[76:79], v[180:183], v[32:47]\n"
  "v_max3_u32 v5, v5, v198, v200\n"
  "v_max3_u32 v5, v5, v202, v204\n"
  "ds_read_b64_tr_b16 v[76:77], v8 offset:39616\n"
  "ds_read_b64_tr_b16 v[78:79], v8 offset:39840\n"
  "s_waitcnt lgkmcnt(12)\n"
  "v_mfma_f32_32x32x16_bf16 v[16:31], v[80:83], v[184:187], v[16:31]\n"
  "buffer_load_dwordx4 v[232:235], v2, s[12:15], s62 offen offset:2048 sc1\n"
  "ds_read_b64_tr_b16 v[80:81], v9 offset:14336\n"
  "ds_read_b64_tr_b16 v[82:83], v9 offset:14560\n"
  "v_mfma_f32_32x32x16_bf16 v[32:47], v[84:87], v[184:187], v[32:47]\n"
  "v_max3_u32 v5, v5, v206, v206\n"
  "v_cmp_eq_u32 vcc, -1, v5\n"
  "ds_read_b64_tr_b16 v[84:85], v9 offset:14400\n"
  "ds_read_b64_tr_b16 v[86:87], v9 offset:14624\n"
  "s_waitcnt lgkmcnt(12)\n"
  "v_mfma_f32_32x32x16_bf16 v[16:31], v[88:91], v[188:191], v[16:31]\n"
  "buffer_load_dwordx4 v[236:239], v2, s[12:15], s62 offen offset:3072 sc1\n"
  "ds_read_b64_tr_b16 v[88:89], v9 offset:39552\n"
  "ds_read_b64_tr_b16 v[90:91], v9 offset:39776\n"
  "v_mfma_f32_32x32x16_bf16 v[32:47], v[92:95], v[188:191], v[32:47]\n"
  "s_cbranch_vccnz .Lc4_f\n"
  ".Lc4_k:\n"
  "ds_read_b64_tr_b16 v[92:93], v9 offset:39616\n"
  "ds_read_b64_tr_b16 v[94:95], v9 offset:39840\n"
  "s_waitcnt lgkmcnt(12)\n"
  "v_mfma_f32_32x32x16_bf16 v[16:31], v[64:67], v[192:195], v[16:31]\n"
  "buffer_load_dwordx4 v[48:51], v14, s[12:15], s91 offen offset:0 sc1\n"
  "ds_read_b64_tr_b16 v[64:65], v8 offset:17920\n"
  "ds_read_b64_tr_b16 v[66:67], v8 offset:18144\n"
  "v_mfma_f32_32x32x16_bf16 v[32:47], v[68:71], v[192:195], v[32:47]\n"
  "s_waitcnt vmcnt(5)\n"
  "v_max3_u32 v5, v208, v210, v212\n"
  "ds_read_b64_tr_b16 v[68:69], v8 offset:17984\n"
  "ds_read_b64_tr_b16 v[70:71], v8 offset:18208\n"
  "s_waitcnt lgkmcnt(12)\n"
  "v_mfma_f32_32x32x16_bf16 v[16:31], v[72:75], v[196:199], v[16:31]\n"
  "buffer_load_dwordx4 v[52:55], v14, s[12:15], s91 offen offset:512 sc1\n"
  "ds_read_b64_tr_b16 v[72:73], v8 offset:43136\n"
  "ds_read_b64_tr_b16 v[74:75], v8 offset:43360\n"
  "v_mfma_f32_32x32x16_bf16 v[32:47], v[76:79], v[196:199], v[32:47]\n"
  "v_max3_u32 v5, v5, v214, v216\n"
  "v_max3_u32 v5, v5, v218, v220\n"
  "ds_read_b64_tr_b16 v[76:77], v8 offset:43200\n"
  "ds_read_b64_tr_b16 v[78:79], v8 offset:43424\n"
  "s_waitcnt lgkmcnt(12)\n"
  "v_mfma_f32_32x32x16_bf16 v[16:31], v[80:83], v[200:203], v[16:31]\n"
  "buffer_load_dwordx4 v[56:59], v14, s[12:15], s91 offen offset:1024 sc1\n"
  "ds_read_b64_tr_b16 v[80:81], v9 offset:17920\n"
  "ds_read_b64_tr_b16 v[82:83], v9 offset:18144\n"
  "v_mfma_f32_32x32x16_bf16 v[32:47], v[84:87], v[200:203], v[32:47]\n"
  "v_max3_u32 v5, v5, v222, v222\n"
  "v_cmp_eq_u32 vcc, -1, v5\n"
  "ds_read_b64_tr_b16 v[84:85], v9 offset:17984\n"
  "ds_read_b64_tr_b16 v[86:87], v9 offset:18208\n"
  "s_waitcnt lgkmcnt(12)\n"
  "v_mfma_f32_32x32x16_bf16 v[16:31], v[88:91], v[204:207], v[16:31]\n"
  "buffer_load_dwordx4 v[60:63], v14, s[12:15], s91 offen offset:1536 sc1\n"
  "ds_read_b64_tr_b16 v[88:89], v9 offset:43136\n"
  "ds_read_b64_tr_b16 v[90:91], v9 offset:43360\n"
  "v_mfma_f32_32x32x16_bf16 v[32:47], v[92:95], v[204:207], v[32:47]\n"
  "s_cbranch_vccnz .Lc5_f\n"
  ".Lc5_k:\n"
  "ds_read_b64_tr_b16 v[92:93], v9 offset:43200\n"
  "ds_read_b64_tr_b16 v[94:95], v9 offset:43424\n"
  "s_waitcnt lgkmcnt(12)\n"
  "v_mfma_f32_32x32x16_bf16 v[16:31], v[64:67], v[208:211], v[16:31]\n"
  "buffer_load_dwordx4 v[240:243], v14, s[12:15], s91 offen offset:2048 sc1\n"
  "ds_read_b64_tr_b16 v[64:65], v8 offset:21504\n"
  "ds_read_b64_tr_b16 v[66:67], v8 offset:21728\n"
  "v_mfma_f32_32x32x16_bf16 v[32:47], v[68:71], v[208:211], v[32:47]\n"
  "s_waitcnt vmcnt(5)\n"
  "v_max3_u32 v5, v224, v226, v228\n"
  "ds_read_b64_tr_b16 v[68:69], v8 offset:21568\n"
  "ds_read_b64_tr_b16 v[70:71], v8 offset:21792\n"
  "s_waitcnt lgkmcnt(12)\n"
  "v_mfma_f32_32x32x16_bf16 v[16:31], v[72:75], v[212:215], v[16:31]\n"
  "buffer_load_dwordx4 v[244:247], v14, s[12:15], s91 offen offset:2560 sc1\n"
  "ds_read_b64_tr_b16 v[72:73], v8 offset:46720\n"
  "ds_read_b64_tr_b16 v[74:75], v8 offset:46944\n"
  "v_mfma_f32_32x32x16_bf16 v[32:47], v[76:79], v[212:215], v[32:47]\n"
  "v_max3_u32 v5, v5, v230, v232\n"
  "v_max3_u32 v5, v5, v234, v236\n"
  "ds_read_b64_tr_b16 v[76:77], v8 offset:46784\n"
  "ds_read_b64_tr_b16 v[78:79], v8 offset:47008\n"
  "s_waitcnt lgkmcnt(12)\n"
  "v_mfma_f32_32x32x16_bf16 v[16:31], v[80:83], v[216:219], v[16:31]\n"
  "buffer_load_dwordx4 v[248:251], v14, s[12:15], s91 offen offset:3072 sc1\n"
  "ds_read_b64_tr_b16 v[80:81], v9 offset:21504\n"
  "ds_read_b64_tr_b16 v[82:83], v9 offset:21728\n"
  "v_mfma_f32_32x32x16_bf16 v[32:47], v[84:87], v[216:219], v[32:47]\n"
  "v_max3_u32 v5, v5, v238, v238\n"
  "v_cmp_eq_u32 vcc, -1, v5\n"
  "ds_read_b64_tr_b16 v[84:85], v9 offset:21568\n"
  "ds_read_b64_tr_b16 v[86:87], v9 offset:21792\n"
  "s_waitcnt lgkmcnt(12)\n"
  "v_mfma_f32_32x32x16_bf16 v[16:31], v[88:91], v[220:223], v[16:31]\n"
  "ds_read_b64_tr_b16 v[88:89], v9 offset:46720\n"
  "ds_read_b64_tr_b16 v[90:91], v9 offset:46944\n"
  "v_mfma_f32_32x32x16_bf16 v[32:47], v[92:95], v[220:223], v[32:47]\n"
  "s_cbranch_vccnz .Lc6_f\n"
  ".Lc6_k:\n"
  "ds_read_b64_tr_b16 v[92:93], v9 offset:46784\n"
  "ds_read_b64_tr_b16 v[94:95], v9 offset:47008\n"
  "s_waitcnt lgkmcnt(12)\n"
  "v_mfma_f32_32x32x16_bf16 v[16:31], v[64:67], v[224:227], v[16:31]\n"
  "s_waitcnt vmcnt(0)\n"
  "v_max3_u32 v5, v48, v50, v52\n"
  "v_max3_u32 v5, v5, v54, v56\n"
  "v_max3_u32 v5, v5, v58, v60\n"
  "v_max3_u32 v5, v5, v62, v240\n"
  "ds_read_b64_tr_b16 v[64:65], v8 offset:128\n"
  "ds_read_b64_tr_b16 v[66:67], v8 offset:352\n"
  "v_mfma_f32_32x32x16_bf16 v[32:47], v[68:71], v[224:227], v[32:47]\n"
  "v_max3_u32 v5, v5, v242, v244\n"
  "v_max3_u32 v5, v5, v246, v248\n"
  "v_max3_u32 v5, v5, v250, v250\n"
  "v_cmp_eq_u32 vcc, -1, v5\n"
  "s_cbranch_vccnz .Lc7_f\n"
  ".Lc7_k:\n"
  "ds_read_b64_tr_b16 v[68:69], v8 offset:25344\n"
  "ds_read_b64_tr_b16 v[70:71], v8 offset:25568\n"
  "s_waitcnt lgkmcnt(12)\n"
  "v_mfma_f32_32x32x16_bf16 v[16:31], v[72:75], v[228:231], v[16:31]\n"
  "ds_read_b64_tr_b16 v[72:73], v9 offset:128\n"
  "ds_read_b64_tr_b16 v[74:75], v9 offset:352\n"
  "v_mfma_f32_32x32x16_bf16 v[32:47], v[76:79], v[228:231], v[32:47]\n"
  "v_add_f32 v48, v48, v50\n"
  "v_add_f32 v49, v49, v51\n"
  "v_add_f32 v52, v52, v54\n"
  "v_add_f32 v53, v53, v55\n"
  "v_add_f32 v48, v48, v52\n"
  "v_add_f32 v49, v49, v53\n"
  "ds_read_b64_tr_b16 v[76:77], v9 offset:25344\n"
  "ds_read_b64_tr_b16 v[78:79], v9 offset:25568\n"
  "s_waitcnt lgkmcnt(12)\n"
  "v_mfma_f32_32x32x16_bf16 v[16:31], v[80:83], v[232:235], v[16:31]\n"
  "v_add_f32 v56, v56, v58\n"
  "v_add_f32 v57, v57, v59\n"
  "v_add_f32 v48, v48, v56\n"
  "v_add_f32 v49, v49, v57\n"
  "v_add_f32 v60, v60, v62\n"
  "v_add_f32 v61, v61, v63\n"
  "ds_read_b64_tr_b16 v[80:81], v8 offset:3712\n"
  "ds_read_b64_tr_b16 v[82:83], v8 offset:3936\n"
  "v_mfma_f32_32x32x16_bf16 v[32:47], v[84:87], v[232:235], v[32:47]\n"
  "v_add_f32 v48, v48, v60\n"
  "v_add_f32 v49, v49, v61\n"
  "v_add_f32 v240, v240, v242\n"
  "v_add_f32 v241, v241, v243\n"
  "v_add_f32 v48, v48, v240\n"
  "v_add_f32 v49, v49, v241\n"
  "ds_read_b64_tr_b16 v[84:85], v8 offset:28928\n"
  "ds_read_b64_tr_b16 v[86:87], v8 offset:29152\n"
  "s_waitcnt lgkmcnt(12)\n"
  "v_mfma_f32_32x32x16_bf16 v[16:31], v[88:91], v[236:239], v[16:31]\n"
  "v_add_f32 v244, v244, v246\n"
  "v_add_f32 v245, v245, v247\n"
  "v_add_f32 v48, v48, v244\n"
  "v_add_f32 v49, v49, v245\n"
  "v_add_f32 v248, v248, v250\n"
  "v_add_f32 v249, v249, v251\n"
  "ds_read_b64_tr_b16 v[88:89], v9 offset:3712\n"
  "ds_read_b64_tr_b16 v[90:91], v9 offset:3936\n"
  "v_mfma_f32_32x32x16_bf16 v[32:47], v[92:95], v[236:239], v[32:47]\n"
  "v_add_f32 v48, v48, v248\n"
  "v_add_f32 v49, v49, v249\n"
  "v_mov_b32 v50, v48\n"
  "v_mov_b32 v51, v49\n"
  "s_nop 0\n"
  "v_permlane32_swap_b32 v48, v50\n"
  "v_permlane32_swap_b32 v49, v51\n"
  "v_add_f32 v48, v48, v50\n"
  "v_add_f32 v49, v49, v51\n"
  "v_mul_f32 v48, -2.2321428571e-03, v48\n"
  "v_mul_f32 v49, 2.2321428571e-03, v49\n"
  "v_fma_f32 v49, -v48, v48, v49\n"
  "v_cvt_pk_bf16_f32 v252, v48, v48\n"
  "v_max_f32 v49, 0, v49\n"
  "v_and_b32 v50, 0xffff0000, v252\n"
  "v_add_f32 v49, 0x3727c5ac, v49\n"
  "v_sub_f32 v50, v48, v50\n"
  "v_rsq_f32 v10, v49\n"
  "v_cvt_pk_bf16_f32 v253, v50, v50\n"
  "v_mov_b32 v255, 0\n"
  "v_mul_f32 v49, v49, v10\n"
  "v_cvt_pk_bf16_f32 v50, v49, v49\n"
  "v_and_b32 v50, 0xffff0000, v50\n"
  "v_sub_f32 v50, v49, v50\n"
  "v_cvt_pk_bf16_f32 v254, v49, v50\n"
  "s_nop 1\n"
  "v_mfma_f32_32x32x16_bf16 v[16:31], v[112:115], v[252:255], v[16:31]\n"
  "v_mfma_f32_32x32x16_bf16 v[32:47], v[116:119], v[252:255], v[32:47]\n"
  "s_nop 10\n"
  "v_pk_mul_f32 v[16:17], v[16:17], v[10:11] op_sel_hi:[1,0]\n"
  "v_pk_mul_f32 v[18:19], v[18:19], v[10:11] op_sel_hi:[1,0]\n"
  "v_pk_mul_f32 v[20:21], v[20:21], v[10:11] op_sel_hi:[1,0]\n"
  "v_pk_mul_f32 v[22:23], v[22:23], v[10:11] op_sel_hi:[1,0]\n"
  "v_max_f32 v16, 0, v16\n"
  "v_max_f32 v17, 0, v17\n"
  "v_max_f32 v18, 0, v18\n"
  "v_max_f32 v19, 0, v19\n"
  "v_max_f32 v20, 0, v20\n"
  "v_max_f32 v21, 0, v21\n"
  "v_max_f32 v22, 0, v22\n"
  "v_max_f32 v23, 0, v23\n"
  "v_pk_mul_f32 v[16:17], v[16:17], v[16:17]\n"
  "v_pk_mul_f32 v[18:19], v[18:19], v[18:19]\n"
  "v_pk_mul_f32 v[20:21], v[20:21], v[20:21]\n"
  "v_pk_mul_f32 v[22:23], v[22:23], v[22:23]\n"
  "v_cvt_pk_bf16_f32 v16, v16, v17\n"
  "v_cvt_pk_bf16_f32 v17, v18, v19\n"
  "v_cvt_pk_bf16_f32 v18, v20, v21\n"
  "v_cvt_pk_bf16_f32 v19, v22, v23\n"
  "v_or_b32 v16, s50, v16\n"
  "v_or_b32 v17, s51, v17\n"
  "v_or_b32 v18, s93, v18\n"
  "v_or_b32 v19, s101, v19\n"
  "buffer_store_dwordx4 v[16:19], v2, s[12:15], s82 offen offset:0 sc1\n"
  "v_pk_mul_f32 v[24:25], v[24:25], v[10:11] op_sel_hi:[1,0]\n"
  "v_pk_mul_f32 v[26:27], v[26:27], v[10:11] op_sel_hi:[1,0]\n"
  "v_pk_mul_f32 v[28:29], v[28:29], v[10:11] op_sel_hi:[1,0]\n"
  "v_pk_mul_f32 v[30:31], v[30:31], v[10:11] op_sel_hi:[1,0]\n"
  "v_max_f32 v24, 0, v24\n"
  "v_max_f32 v25, 0, v25\n"
  "v_max_f32 v26, 0, v26\n"
  "v_max_f32 v27, 0, v27\n"
  "v_max_f32 v28, 0, v28\n"
  "v_max_f32 v29, 0, v29\n"
  "v_max_f32 v30, 0, v30\n"
  "v_max_f32 v31, 0, v31\n"
  "v_pk_mul_f32 v[24:25], v[24:25], v[24:25]\n"
  "v_pk_mul_f32 v[26:27], v[26:27], v[26:27]\n"
  "v_pk_mul_f32 v[28:29], v[28:29], v[28:29]\n"
  "v_pk_mul_f32 v[30:31], v[30:31], v[30:31]\n"
  "v_cvt_pk_bf16_f32 v24, v24, v25\n"
  "v_cvt_pk_bf16_f32 v25, v26, v27\n"
  "v_cvt_pk_bf16_f32 v26, v28, v29\n"
  "v_cvt_pk_bf16_f32 v27, v30, v31\n"
  "v_or_b32 v24, s50, v24\n"
  "v_or_b32 v25, s51, v25\n"
  "v_or_b32 v26, s93, v26\n"
  "v_or_b32 v27, s101, v27\n"
  "buffer_store_dwordx4 v[24:27], v2, s[12:15], s82 offen offset:1024 sc1\n"
  "ds_read_b64_tr_b16 v[92:93], v9 offset:28928\n"
  "ds_read_b64_tr_b16 v[94:95], v9 offset:29152\n"
  "s_waitcnt lgkmcnt(12)\n"
  "v_mfma_f32_32x32x16_bf16 v[48:63], v[64:67], v[128:131], 0\n"
  "s_add_u32 s67, s57, 2\n"
  "s_and_b32 s67, s67, 3\n"
  "s_mul_i32 s67, s67, 14336\n"
  "s_add_u32 s67, s67, s96\n"
  "s_add_u32 s67, s67, 12288\n"
  "buffer_load_dwordx4 a[208:211], v12, s[12:15], s67 offen offset:0 sc1\n"
  "ds_read_b64_tr_b16 v[64:65], v8 offset:7296\n"
  "ds_read_b64_tr_b16 v[66:67], v8 offset:7520\n"
  "v_mfma_f32_32x32x16_bf16 v[48:63], v[68:71], v[132:135], v[48:63]\n"
  "v_mul_f32 v32, v32, v10\n"
  "v_mul_f32 v33, v33, v10\n"
  "v_mul_f32 v34, v34, v10\n"
  "ds_read_b64_tr_b16 v[68:69], v8 offset:32512\n"
  "ds_read_b64_tr_b16 v[70:71], v8 offset:32736\n"
  "s_waitcnt lgkmcnt(12)\n"
  "v_mfma_f32_32x32x16_bf16 v[48:63], v[72:75], v[136:139], v[48:63]\n"
  "v_mul_f32 v35, v35, v10\n"
  "v_mul_f32 v36, v36, v10\n"
  "buffer_load_dwordx4 a[212:215], v13, s[12:15], s67 offen offset:0 sc1\n"
  "ds_read_b64_tr_b16 v[72:73], v9 offset:7296\n"
  "ds_read_b64_tr_b16 v[74:75], v9 offset:7520\n"
  "v_mfma_f32_32x32x16_bf16 v[48:63], v[76:79], v[140:143], v[48:63]\n"
  "v_mul_f32 v37, v37, v10\n"
  "v_mul_f32 v38, v38, v10\n"
  "v_mul_f32 v39, v39, v10\n"
  "ds_read_b64_tr_b16 v[76:77], v9 offset:32512\n"
  "ds_read_b64_tr_b16 v[78:79], v9 offset:32736\n"
  "s_waitcnt lgkmcnt(12)\n"
  "v_mfma_f32_32x32x16_bf16 v[48:63], v[80:83], v[144:147], v[48:63]\n"
  "v_max_f32 v32, 0, v32\n"
  "v_max_f32 v33, 0, v33\n"
  "s_add_u32 s67, s57, 3\n"
  "s_and_b32 s67, s67, 3\n"
  "s_mul_i32 s67, s67, 14336\n"
  "s_add_u32 s67, s67, s96\n"
  "s_add_u32 s67, s67, 12288\n"
  "buffer_load_dwordx4 a[216:219], v12, s[12:15], s67 offen offset:0 sc1\n"
  "ds_read_b64_tr_b16 v[80:81], v8 offset:10880\n"
  "ds_read_b64_tr_b16 v[82:83], v8 offset:11104\n"
  "v_mfma_f32_32x32x16_bf16 v[48:63], v[84:87], v[148:151], v[48:63]\n"
  "v_max_f32 v34, 0, v34\n"
  "v_max_f32 v35, 0, v35\n"
  "v_max_f32 v36, 0, v36\n"
  "ds_read_b64_tr_b16 v[84:85], v8 offset:36096\n"
  "ds_read_b64_tr_b16 v[86:87], v8 offset:36320\n"
  "s_waitcnt lgkmcnt(12)\n"
  "v_mfma_f32_32x32x16_bf16 v[48:63], v[88:91], v[152:155], v[48:63]\n"
  "v_max_f32 v37, 0, v37\n"
  "v_max_f32 v38, 0, v38\n"
  "buffer_load_dwordx4 a[220:223], v13, s[12:15], s67 offen offset:0 sc1\n"
  "ds_read_b64_tr_b16 v[88:89], v9 offset:10880\n"
  "ds_read_b64_tr_b16 v[90:91], v9 offset:11104\n"
  "v_mfma_f32_32x32x16_bf16 v[48:63], v[92:95], v[156:159], v[48:63]\n"
  "v_max_f32 v39, 0, v39\n"
  "v_mul_f32 v32, v32, v32\n"
  "v_mul_f32 v33, v33, v33\n"
  "ds_read_b64_tr_b16 v[92:93], v9 offset:36096\n"
  "ds_read_b64_tr_b16 v[94:95], v9 offset:36320\n"
  "s_waitcnt lgkmcnt(12)\n"
  "v_mfma_f32_32x32x16_bf16 v[48:63], v[64:67], v[160:163], v[48:63]\n"
  "v_mul_f32 v34, v34, v34\n"
  "v_mul_f32 v35, v35, v35\n"
  "ds_read_b64_tr_b16 v[64:65], v8 offset:14464\n"
  "ds_read_b64_tr_b16 v[66:67], v8 offset:14688\n"
  "v_mfma_f32_32x32x16_bf16 v[48:63], v[68:71], v[164:167], v[48:63]\n"
  "v_mul_f32 v36, v36, v36\n"
  "v_mul_f32 v37, v37, v37\n"
  "v_mul_f32 v38, v38, v38\n"
  "ds_read_b64_tr_b16 v[68:69], v8 offset:39680\n"
  "ds_read_b64_tr_b16 v[70:71], v8 offset:39904\n"
  "s_waitcnt lgkmcnt(12)\n"
  "v_mfma_f32_32x32x16_bf16 v[48:63], v[72:75], v[168:171], v[48:63]\n"
  "v_mul_f32 v39, v39, v39\n"
  "v_cvt_pk_bf16_f32 v32, v32, v33\n"
  "ds_read_b64_tr_b16 v[72:73], v9 offset:14464\n"
  "ds_read_b64_tr_b16 v[74:75], v9 offset:14688\n"
  "v_mfma_f32_32x32x16_bf16 v[48:63], v[76:79], v[172:175], v[48:63]\n"
  "v_cvt_pk_bf16_f32 v33, v34, v35\n"
  "v_cvt_pk_bf16_f32 v34, v36, v37\n"
  "v_cvt_pk_bf16_f32 v35, v38, v39\n"
  "ds_read_b64_tr_b16 v[76:77], v9 offset:39680\n"
  "ds_read_b64_tr_b16 v[78:79], v9 offset:39904\n"
  "s_waitcnt lgkmcnt(12)\n"
  "v_mfma_f32_32x32x16_bf16 v[48:63], v[80:83], v[176:179], v[48:63]\n"
  "v_or_b32 v32, s50, v32\n"
  "v_or_b32 v33, s51, v33\n"
  "ds_read_b64_tr_b16 v[80:81], v8 offset:18048\n"
  "ds_read_b64_tr_b16 v[82:83], v8 offset:18272\n"
  "v_mfma_f32_32x32x16_bf16 v[48:63], v[84:87], v[180:183], v[48:63]\n"
  "v_or_b32 v34, s93, v34\n"
  "v_or_b32 v35, s101, v35\n"
  "buffer_store_dwordx4 v[32:35], v2, s[12:15], s82 offen offset:2048 sc1\n"
  "ds_read_b64_tr_b16 v[84:85], v8 offset:43264\n"
  "ds_read_b64_tr_b16 v[86:87], v8 offset:43488\n"
  "s_waitcnt lgkmcnt(12)\n"
  "v_mfma_f32_32x32x16_bf16 v[48:63], v[88:91], v[184:187], v[48:63]\n"
  "v_mul_f32 v40, v40, v10\n"
  "v_mul_f32 v41, v41, v10\n"
  "ds_read_b64_tr_b16 v[88:89], v9 offset:18048\n"
  "ds_read_b64_tr_b16 v[90:91], v9 offset:18272\n"
  "v_mfma_f32_32x32x16_bf16 v[48:63], v[92:95], v[188:191], v[48:63]\n"
  "v_mul_f32 v42, v42, v10\n"
  "v_mul_f32 v43, v43, v10\n"
  "v_mul_f32 v44, v44, v10\n"
  "ds_read_b64_tr_b16 v[92:93], v9 offset:43264\n"
  "ds_read_b64_tr_b16 v[94:95], v9 offset:43488\n"
  "s_waitcnt lgkmcnt(12)\n"
  "v_mfma_f32_32x32x16_bf16 v[48:63], v[64:67], v[192:195], v[48:63]\n"
  "v_mul_f32 v45, v45, v10\n"
  "v_mul_f32 v46, v46, v10\n"
  "ds_read_b64_tr_b16 v[64:65], v8 offset:21632\n"
  "ds_read_b64_tr_b16 v[66:67], v8 offset:21856\n"
  "v_mfma_f32_32x32x16_bf16 v[48:63], v[68:71], v[196:199], v[48:63]\n"
  "v_mul_f32 v47, v47, v10\n"
  "v_max_f32 v40, 0, v40\n"
  "v_max_f32 v41, 0, v41\n"
  "ds_read_b64_tr_b16 v[68:69], v8 offset:46848\n"
  "ds_read_b64_tr_b16 v[70:71], v8 offset:47072\n"
  "s_waitcnt lgkmcnt(12)\n"
  "v_mfma_f32_32x32x16_bf16 v[48:63], v[72:75], v[200:203], v[48:63]\n"
  "v_max_f32 v42, 0, v42\n"
  "v_max_f32 v43, 0, v43\n"
  "ds_read_b64_tr_b16 v[72:73], v9 offset:21632\n"
  "ds_read_b64_tr_b16 v[74:75], v9 offset:21856\n"
  "v_mfma_f32_32x32x16_bf16 v[48:63], v[76:79], v[204:207], v[48:63]\n"
  "v_max_f32 v44, 0, v44\n"
  "v_max_f32 v45, 0, v45\n"
  "v_max_f32 v46, 0, v46\n"
  "ds_read_b64_tr_b16 v[76:77], v9 offset:46848\n"
  "ds_read_b64_tr_b16 v[78:79], v9 offset:47072\n"
  "s_waitcnt lgkmcnt(12)\n"
  "v_mfma_f32_32x32x16_bf16 v[48:63], v[80:83], v[208:211], v[48:63]\n"
  "v_max_f32 v47, 0, v47\n"
  "v_mul_f32 v40, v40, v40\n"
  "ds_read_b64_tr_b16 v[80:81], v8 offset:192\n"
  "ds_read_b64_tr_b16 v[82:83], v8 offset:416\n"
  "v_mfma_f32_32x32x16_bf16 v[48:63], v[84:87], v[212:215], v[48:63]\n"
  "v_mul_f32 v41, v41, v41\n"
  "v_mul_f32 v42, v42, v42\n"
  "v_mul_f32 v43, v43, v43\n"
  "ds_read_b64_tr_b16 v[84:85], v8 offset:25408\n"
  "ds_read_b64_tr_b16 v[86:87], v8 offset:25632\n"
  "s_waitcnt lgkmcnt(12)\n"
  "v_mfma_f32_32x32x16_bf16 v[48:63], v[88:91], v[216:219], v[48:63]\n"
  "v_mul_f32 v44, v44, v44\n"
  "v_mul_f32 v45, v45, v45\n"
  "ds_read_b64_tr_b16 v[88:89], v9 offset:192\n"
  "ds_read_b64_tr_b16 v[90:91], v9 offset:416\n"
  "v_mfma_f32_32x32x16_bf16 v[48:63], v[92:95], v[220:223], v[48:63]\n"
  "v_mul_f32 v46, v46, v46\n"
  "v_mul_f32 v47, v47, v47\n"
  "v_cvt_pk_bf16_f32 v40, v40, v41\n"
  "ds_read_b64_tr_b16 v[92:93], v9 offset:25408\n"
  "ds_read_b64_tr_b16 v[94:95], v9 offset:25632\n"
  "s_waitcnt lgkmcnt(12)\n"
  "v_mfma_f32_32x32x16_bf16 v[48:63], v[64:67], v[224:227], v[48:63]\n"
  "v_cvt_pk_bf16_f32 v41, v42, v43\n"
  "v_cvt_pk_bf16_f32 v42, v44, v45\n"
  "ds_read_b64_tr_b16 v[64:65], v8 offset:3776\n"
  "ds_read_b64_tr_b16 v[66:67], v8 offset:4000\n"
  "v_mfma_f32_32x32x16_bf16 v[48:63], v[68:71], v[228:231], v[48:63]\n"
  "v_cvt_pk_bf16_f32 v43, v46, v47\n"
  "v_or_b32 v40, s50, v40\n"
  "v_or_b32 v41, s51, v41\n"
  "ds_read_b64_tr_b16 v[68:69], v8 offset:28992\n"
  "ds_read_b64_tr_b16 v[70:71], v8 offset:29216\n"
  "s_waitcnt lgkmcnt(12)\n"
  "v_mfma_f32_32x32x16_bf16 v[48:63], v[72:75], v[232:235], v[48:63]\n"
  "v_or_b32 v42, s93, v42\n"
  "v_or_b32 v43, s101, v43\n"
  "ds_read_b64_tr_b16 v[72:73], v9 offset:3776\n"
  "ds_read_b64_tr_b16 v[74:75], v9 offset:4000\n"
  "v_mfma_f32_32x32x16_bf16 v[48:63], v[76:79], v[236:239], v[48:63]\n"
  "buffer_store_dwordx4 v[40:43], v2, s[12:15], s82 offen offset:3072 sc1\n"
  "v_mfma_f32_32x32x16_bf16 v[48:63], v[120:123], v[252:255], v[48:63]\n"
  "s_mul_i32 s58, s83, 112\n"
  "s_mul_i32 s59, s52, 28\n"
  "s_add_u32 s58, s58, s59\n"
  "s_lshl_b32 s58, s58, 10\n"
  "s_add_u32 s58, s58, 7512064\n"
  "s_add_u32 s92, s58, s3\n"
  "ds_read_b64_tr_b16 v[76:77], v9 offset:28992\n"
  "ds_read_b64_tr_b16 v[78:79], v9 offset:29216\n"
  "s_waitcnt lgkmcnt(12)\n"
  "v_mfma_f32_32x32x16_bf16 v[16:31], v[80:83], v[128:131], 0\n"
  "ds_read_b64_tr_b16 v[80:81], v8 offset:7360\n"
  "ds_read_b64_tr_b16 v[82:83], v8 offset:7584\n"
  "v_mfma_f32_32x32x16_bf16 v[16:31], v[84:87], v[132:135], v[16:31]\n"
  "v_mul_f32 v48, v48, v10\n"
  "v_mul_f32 v49, v49, v10\n"
  "v_mul_f32 v50, v50, v10\n"
  "ds_read_b64_tr_b16 v[84:85], v8 offset:32576\n"
  "ds_read_b64_tr_b16 v[86:87], v8 offset:32800\n"
  "s_waitcnt lgkmcnt(12)\n"
  "v_mfma_f32_32x32x16_bf16 v[16:31], v[88:91], v[136:139], v[16:31]\n"
  "v_mul_f32 v51, v51, v10\n"
  "v_mul_f32 v52, v52, v10\n"
  "ds_read_b64_tr_b16 v[88:89], v9 offset:7360\n"
  "ds_read_b64_tr_b16 v[90:91], v9 offset:7584\n"
  "v_mfma_f32_32x32x16_bf16 v[16:31], v[92:95], v[140:143], v[16:31]\n"
  "v_mul_f32 v53, v53, v10\n"
  "v_mul_f32 v54, v54, v10\n"
  "v_mul_f32 v55, v55, v10\n"
  "ds_read_b64_tr_b16 v[92:93], v9 offset:32576\n"
  "ds_read_b64_tr_b16 v[94:95], v9 offset:32800\n"
  "s_waitcnt lgkmcnt(12)\n"
  "v_mfma_f32_32x32x16_bf16 v[16:31], v[64:67], v[144:147], v[16:31]\n"
  "v_max_f32 v48, 0, v48\n"
  "v_max_f32 v49, 0, v49\n"
  "ds_read_b64_tr_b16 v[64:65], v8 offset:10944\n"
  "ds_read_b64_tr_b16 v[66:67], v8 offset:11168\n"
  "v_mfma_f32_32x32x16_bf16 v[16:31], v[68:71], v[148:151], v[16:31]\n"
  "v_max_f32 v50, 0, v50\n"
  "v_max_f32 v51, 0, v51\n"
  "v_max_f32 v52, 0, v52\n"
  "ds_read_b64_tr_b16 v[68:69], v8 offset:36160\n"
  "ds_read_b64_tr_b16 v[70:71], v8 offset:36384\n"
  "s_waitcnt lgkmcnt(12)\n"
  "v_mfma_f32_32x32x16_bf16 v[16:31], v[72:75], v[152:155], v[16:31]\n"
  "v_max_f32 v53, 0, v53\n"
  "v_max_f32 v54, 0, v54\n"
  "ds_read_b64_tr_b16 v[72:73], v9 offset:10944\n"
  "ds_read_b64_tr_b16 v[74:75], v9 offset:11168\n"
  "v_mfma_f32_32x32x16_bf16 v[16:31], v[76:79], v[156:159], v[16:31]\n"
  "v_max_f32 v55, 0, v55\n"
  "v_mul_f32 v48, v48, v48\n"
  "v_mul_f32 v49, v49, v49\n"
  "ds_read_b64_tr_b16 v[76:77], v9 offset:36160\n"
  "ds_read_b64_tr_b16 v[78:79], v9 offset:36384\n"
  "s_waitcnt lgkmcnt(12)\n"
  "v_mfma_f32_32x32x16_bf16 v[16:31], v[80:83], v[160:163], v[16:31]\n"
  "v_mul_f32 v50, v50, v50\n"
  "v_mul_f32 v51, v51, v51\n"
  "ds_read_b64_tr_b16 v[80:81], v8 offset:14528\n"
  "ds_read_b64_tr_b16 v[82:83], v8 offset:14752\n"
  "v_mfma_f32_32x32x16_bf16 v[16:31], v[84:87], v[164:167], v[16:31]\n"
  "v_mul_f32 v52, v52, v52\n"
  "v_mul_f32 v53, v53, v53\n"
  "v_mul_f32 v54, v54, v54\n"
  "ds_read_b64_tr_b16 v[84:85], v8 offset:39744\n"
  "ds_read_b64_tr_b16 v[86:87], v8 offset:39968\n"
  "s_waitcnt lgkmcnt(12)\n"
  "v_mfma_f32_32x32x16_bf16 v[16:31], v[88:91], v[168:171], v[16:31]\n"
  "v_mul_f32 v55, v55, v55\n"
  "v_cvt_pk_bf16_f32 v48, v48, v49\n"
  "ds_read_b64_tr_b16 v[88:89], v9 offset:14528\n"
  "ds_read_b64_tr_b16 v[90:91], v9 offset:14752\n"
  "v_mfma_f32_32x32x16_bf16 v[16:31], v[92:95], v[172:175], v[16:31]\n"
  "v_cvt_pk_bf16_f32 v49, v50, v51\n"
  "v_cvt_pk_bf16_f32 v50, v52, v53\n"
  "v_cvt_pk_bf16_f32 v51, v54, v55\n"
  "ds_read_b64_tr_b16 v[92:93], v9 offset:39744\n"
  "ds_read_b64_tr_b16 v[94:95], v9 offset:39968\n"
  "s_waitcnt lgkmcnt(12)\n"
  "v_mfma_f32_32x32x16_bf16 v[16:31], v[64:67], v[176:179], v[16:31]\n"
  "v_or_b32 v48, s50, v48\n"
  "v_or_b32 v49, s51, v49\n"
  "ds_read_b64_tr_b16 v[64:65], v8 offset:18112\n"
  "ds_read_b64_tr_b16 v[66:67], v8 offset:18336\n"
  "v_mfma_f32_32x32x16_bf16 v[16:31], v[68:71], v[180:183], v[16:31]\n"
  "v_or_b32 v50, s93, v50\n"
  "v_or_b32 v51, s101, v51\n"
  "s_add_u32 s61, s82, 4096\n"
  "buffer_store_dwordx4 v[48:51], v2, s[12:15], s61 offen offset:0 sc1\n"
  "ds_read_b64_tr_b16 v[68:69], v8 offset:43328\n"
  "ds_read_b64_tr_b16 v[70:71], v8 offset:43552\n"
  "s_waitcnt lgkmcnt(12)\n"
  "v_mfma_f32_32x32x16_bf16 v[16:31], v[72:75], v[184:187], v[16:31]\n"
  "v_mul_f32 v56, v56, v10\n"
  "v_mul_f32 v57, v57, v10\n"
  "ds_read_b64_tr_b16 v[72:73], v9 offset:18112\n"
  "ds_read_b64_tr_b16 v[74:75], v9 offset:18336\n"
  "v_mfma_f32_32x32x16_bf16 v[16:31], v[76:79], v[188:191], v[16:31]\n"
  "v_mul_f32 v58, v58, v10\n"
  "v_mul_f32 v59, v59, v10\n"
  "v_mul_f32 v60, v60, v10\n"
  "ds_read_b64_tr_b16 v[76:77], v9 offset:43328\n"
  "ds_read_b64_tr_b16 v[78:79], v9 offset:43552\n"
  "s_waitcnt lgkmcnt(12)\n"
  "v_mfma_f32_32x32x16_bf16 v[16:31], v[80:83], v[192:195], v[16:31]\n"
  "v_mul_f32 v61, v61, v10\n"
  "v_mul_f32 v62, v62, v10\n"
  "ds_read_b64_tr_b16 v[80:81], v8 offset:21696\n"
  "ds_read_b64_tr_b16 v[82:83], v8 offset:21920\n"
  "v_mfma_f32_32x32x16_bf16 v[16:31], v[84:87], v[196:199], v[16:31]\n"
  "v_mul_f32 v63, v63, v10\n"
  "v_max_f32 v56, 0, v56\n"
  "v_max_f32 v57, 0, v57\n"
  "ds_read_b64_tr_b16 v[84:85], v8 offset:46912\n"
  "ds_read_b64_tr_b16 v[86:87], v8 offset:47136\n"
  "s_waitcnt lgkmcnt(12)\n"
  "v_mfma_f32_32x32x16_bf16 v[16:31], v[88:91], v[200:203], v[16:31]\n"
  "v_max_f32 v58, 0, v58\n"
  "v_max_f32 v59, 0, v59\n"
  "ds_read_b64_tr_b16 v[88:89], v9 offset:21696\n"
  "ds_read_b64_tr_b16 v[90:91], v9 offset:21920\n"
  "v_mfma_f32_32x32x16_bf16 v[16:31], v[92:95], v[204:207], v[16:31]\n"
  "v_max_f32 v60, 0, v60\n"
  "v_max_f32 v61, 0, v61\n"
  "v_max_f32 v62, 0, v62\n"
  "ds_read_b64_tr_b16 v[92:93], v9 offset:46912\n"
  "ds_read_b64_tr_b16 v[94:95], v9 offset:47136\n"
  "s_waitcnt lgkmcnt(12)\n"
  "v_mfma_f32_32x32x16_bf16 v[16:31], v[64:67], v[208:211], v[16:31]\n"
  "v_max_f32 v63, 0, v63\n"
  "v_mul_f32 v56, v56, v56\n"
  "v_mfma_f32_32x32x16_bf16 v[16:31], v[68:71], v[212:215], v[16:31]\n"
  "v_mul_f32 v57, v57, v57\n"
  "v_mul_f32 v58, v58, v58\n"
  "v_mul_f32 v59, v59, v59\n"
  "s_waitcnt lgkmcnt(8)\n"
  "v_mfma_f32_32x32x16_bf16 v[16:31], v[72:75], v[216:219], v[16:31]\n"
  "v_mul_f32 v60, v60, v60\n"
  "v_mul_f32 v61, v61, v61\n"
  "v_mfma_f32_32x32x16_bf16 v[16:31], v[76:79], v[220:223], v[16:31]\n"
  "v_mul_f32 v62, v62, v62\n"
  "v_mul_f32 v63, v63, v63\n"
  "v_cvt_pk_bf16_f32 v56, v56, v57\n"
  "s_waitcnt lgkmcnt(4)\n"
  "v_mfma_f32_32x32x16_bf16 v[16:31], v[80:83], v[224:227], v[16:31]\n"
  "v_cvt_pk_bf16_f32 v57, v58, v59\n"
  "v_cvt_pk_bf16_f32 v58, v60, v61\n"
  "v_mfma_f32_32x32x16_bf16 v[16:31], v[84:87], v[228:231], v[16:31]\n"
  "v_cvt_pk_bf16_f32 v59, v62, v63\n"
  "v_or_b32 v56, s50, v56\n"
  "v_or_b32 v57, s51, v57\n"
  "s_waitcnt lgkmcnt(0)\n"
  "v_mfma_f32_32x32x16_bf16 v[16:31], v[88:91], v[232:235], v[16:31]\n"
  "v_or_b32 v58, s93, v58\n"
  "v_or_b32 v59, s101, v59\n"
  "v_mfma_f32_32x32x16_bf16 v[16:31], v[92:95], v[236:239], v[16:31]\n"
  "s_add_u32 s61, s82, 4096\n"
  "buffer_store_dwordx4 v[56:59], v2, s[12:15], s61 offen offset:1024 sc1\n"
  "v_mfma_f32_32x32x16_bf16 v[16:31], v[124:127], v[252:255], v[16:31]\n"
  "s_nop 11\n"
  "v_pk_mul_f32 v[16:17], v[16:17], v[10:11] op_sel_hi:[1,0]\n"
  "v_pk_mul_f32 v[18:19], v[18:19], v[10:11] op_sel_hi:[1,0]\n"
  "v_pk_mul_f32 v[20:21], v[20:21], v[10:11] op_sel_hi:[1,0]\n"
  "v_pk_mul_f32 v[22:23], v[22:23], v[10:11] op_sel_hi:[1,0]\n"
  "v_max_f32 v16, 0, v16\n"
  "v_max_f32 v17, 0, v17\n"
  "v_max_f32 v18, 0, v18\n"
  "v_max_f32 v19, 0, v19\n"
  "v_max_f32 v20, 0, v20\n"
  "v_max_f32 v21, 0, v21\n"
  "v_max_f32 v22, 0, v22\n"
  "v_max_f32 v23, 0, v23\n"
  "v_pk_mul_f32 v[16:17], v[16:17], v[16:17]\n"
  "v_pk_mul_f32 v[18:19], v[18:19], v[18:19]\n"
  "v_pk_mul_f32 v[20:21], v[20:21], v[20:21]\n"
  "v_pk_mul_f32 v[22:23], v[22:23], v[22:23]\n"
  "v_cvt_pk_bf16_f32 v16, v16, v17\n"
  "v_cvt_pk_bf16_f32 v17, v18, v19\n"
  "v_cvt_pk_bf16_f32 v18, v20, v21\n"
  "v_cvt_pk_bf16_f32 v19, v22, v23\n"
  "v_or_b32 v16, s50, v16\n"
  "v_or_b32 v17, s51, v17\n"
  "v_or_b32 v18, s93, v18\n"
  "v_or_b32 v19, s101, v19\n"
  "s_add_u32 s61, s82, 4096\n"
  "buffer_store_dwordx4 v[16:19], v2, s[12:15], s61 offen offset:2048 sc1\n"
  "s_add_u32 s62, s57, 0\n"
  "s_and_b32 s62, s62, 3\n"
  "s_mul_i32 s62, s62, 7168\n"
  "s_add_u32 s62, s62, s92\n"
  "buffer_load_dwordx4 v[240:243], v2, s[12:15], s62 offen offset:0 sc1\n"
  "buffer_load_dwordx4 v[244:247], v2, s[12:15], s62 offen offset:1024 sc1\n"
  "buffer_load_dwordx4 v[248:251], v11, s[12:15], s62 offen offset:0 sc1\n"
  "buffer_load_dwordx4 v[112:115], v11, s[12:15], s62 offen offset:1024 sc1\n"
  "s_add_u32 s62, s57, 1\n"
  "s_and_b32 s62, s62, 3\n"
  "s_mul_i32 s62, s62, 7168\n"
  "s_add_u32 s62, s62, s92\n"
  "buffer_load_dwordx4 v[116:119], v2, s[12:15], s62 offen offset:0 sc1\n"
  "buffer_load_dwordx4 v[120:123], v2, s[12:15], s62 offen offset:1024 sc1\n"
  "buffer_load_dwordx4 v[128:131], v11, s[12:15], s62 offen offset:0 sc1\n"
  "buffer_load_dwordx4 v[132:135], v11, s[12:15], s62 offen offset:1024 sc1\n"
  "s_waitcnt vmcnt(8)\n"
  "s_add_u32 s62, s57, 2\n"
  "s_and_b32 s62, s62, 3\n"
  "s_mul_i32 s62, s62, 7168\n"
  "s_add_u32 s62, s62, s92\n"
  "buffer_load_dwordx4 v[136:139], v2, s[12:15], s62 offen offset:0 sc1\n"
  "buffer_load_dwordx4 v[140:143], v2, s[12:15], s62 offen offset:1024 sc1\n"
  "buffer_load_dwordx4 v[144:147], v11, s[12:15], s62 offen offset:0 sc1\n"
  "buffer_load_dwordx4 v[148:151], v11, s[12:15], s62 offen offset:1024 sc1\n"
  "s_add_u32 s62, s57, 3\n"
  "s_and_b32 s62, s62, 3\n"
  "s_mul_i32 s62, s62, 7168\n"
  "s_add_u32 s62, s62, s92\n"
  "buffer_load_dwordx4 v[152:155], v2, s[12:15], s62 offen offset:0 sc1\n"
  "buffer_load_dwordx4 v[156:159], v2, s[12:15], s62 offen offset:1024 sc1\n"
  "buffer_load_dwordx4 v[160:163], v11, s[12:15], s62 offen offset:0 sc1\n"
  "buffer_load_dwordx4 v[164:167], v11, s[12:15], s62 offen offset:1024 sc1\n"
  "s_cmp_eq_u32 s3, 0\n"
  "s_cselect_b32 s50, s50, 0\n"
  "s_cselect_b32 s51, s51, 0\n"
  "s_cselect_b32 s93, s93, 0\n"
  "s_cselect_b32 s101, s101, 0\n"
  "s_xor_b32 s58, s52, 1\n"
  "s_mul_i32 s58, s58, 6144\n"
  "s_add_u32 s58, s58, 100864\n"
  "v_add_u32 v124, s58, v2\n"
  "s_xor_b32 s58, s52, 2\n"
  "s_mul_i32 s58, s58, 6144\n"
  "s_add_u32 s58, s58, 102912\n"
  "v_add_u32 v125, s58, v2\n"
  "s_xor_b32 s58, s52, 3\n"
  "s_mul_i32 s58, s58, 6144\n"
  "s_add_u32 s58, s58, 104960\n"
  "v_add_u32 v126, s58, v2\n"
  "s_mul_i32 s58, s52, 6144\n"
  "s_add_u32 s58, s58, 100864\n"
  "v_add_u32 v127, s58, v2\n"
  "s_waitcnt vmcnt(12)\n"
  "v_xor_b32 v5, s50, v240\n"
  "v_bitop3_b32 v5, v241, s51, v5 bitop3:0xbe\n"
  "v_bitop3_b32 v5, v242, s93, v5 bitop3:0xbe\n"
  "v_bitop3_b32 v5, v243, s101, v5 bitop3:0xbe\n"
  "v_bitop3_b32 v5, v244, s50, v5 bitop3:0xbe\n"
  "v_bitop3_b32 v5, v245, s51, v5 bitop3:0xbe\n"
  "v_bitop3_b32 v5, v246, s93, v5 bitop3:0xbe\n"
  "v_bitop3_b32 v5, v247, s101, v5 bitop3:0xbe\n"
  "v_bitop3_b32 v5, v248, s50, v5 bitop3:0xbe\n"
  "v_bitop3_b32 v5, v249, s51, v5 bitop3:0xbe\n"
  "v_bitop3_b32 v5, v250, s93, v5 bitop3:0xbe\n"
  "v_bitop3_b32 v5, v251, s101, v5 bitop3:0xbe\n"
  "v_bitop3_b32 v5, v112, s50, v5 bitop3:0xbe\n"
  "v_bitop3_b32 v5, v113, s51, v5 bitop3:0xbe\n"
  "v_bitop3_b32 v5, v114, s93, v5 bitop3:0xbe\n"
  "v_bitop3_b32 v5, v115, s101, v5 bitop3:0xbe\n"
  "v_and_b32 v5, 0x80008000, v5\n"
  "v_cmp_ne_u32 vcc, 0, v5\n"
  "s_cbranch_vccnz .Ld0_0_f\n"
  ".Ld0_0_k:\n"
  "v_mfma_f32_32x32x16_bf16 a[224:239], a[0:3], v[240:243], 0\n"
  "s_add_u32 s62, s57, 0\n"
  "s_and_b32 s62, s62, 3\n"
  "s_mul_i32 s62, s62, 7168\n"
  "s_add_u32 s62, s62, s92\n"
  "buffer_load_dwordx4 v[168:171], v2, s[12:15], s62 offen offset:2048 sc1\n"
  "s_waitcnt vmcnt(9)\n"
  "v_mfma_f32_32x32x16_bf16 a[240:255], a[4:7], v[240:243], 0\n"
  "v_xor_b32 v5, s50, v116\n"
  "v_bitop3_b32 v5, v117, s51, v5 bitop3:0xbe\n"
  "v_bitop3_b32 v5, v118, s93, v5 bitop3:0xbe\n"
  "v_bitop3_b32 v5, v119, s101, v5 bitop3:0xbe\n"
  "v_mfma_f32_32x32x16_bf16 a[224:239], a[32:35], v[244:247], a[224:239]\n"
  "buffer_load_dwordx4 v[172:175], v2, s[12:15], s62 offen offset:3072 sc1\n"
  "v_bitop3_b32 v5, v120, s50, v5 bitop3:0xbe\n"
  "v_bitop3_b32 v5, v121, s51, v5 bitop3:0xbe\n"
  "v_mfma_f32_32x32x16_bf16 a[240:255], a[36:39], v[244:247], a[240:255]\n"
  "v_bitop3_b32 v5, v122, s93, v5 bitop3:0xbe\n"
  "v_bitop3_b32 v5, v123, s101, v5 bitop3:0xbe\n"
  "v_bitop3_b32 v5, v128, s50, v5 bitop3:0xbe\n"
  "v_bitop3_b32 v5, v129, s51, v5 bitop3:0xbe\n"
  "v_mfma_f32_32x32x16_bf16 v[16:31], a[0:3], v[248:251], 0\n"
  "buffer_load_dwordx4 v[176:179], v11, s[12:15], s62 offen offset:2048 sc1\n"
  "v_bitop3_b32 v5, v130, s93, v5 bitop3:0xbe\n"
  "v_bitop3_b32 v5, v131, s101, v5 bitop3:0xbe\n"
  "v_mfma_f32_32x32x16_bf16 v[32:47], a[4:7], v[248:251], 0\n"
  "v_bitop3_b32 v5, v132, s50, v5 bitop3:0xbe\n"
  "v_bitop3_b32 v5, v133, s51, v5 bitop3:0xbe\n"
  "v_bitop3_b32 v5, v134, s93, v5 bitop3:0xbe\n"
  "v_bitop3_b32 v5, v135, s101, v5 bitop3:0xbe\n"
  "v_mfma_f32_32x32x16_bf16 v[16:31], a[32:35], v[112:115], v[16:31]\n"
  "buffer_load_dwordx4 v[180:183], v11, s[12:15], s62 offen offset:3072 sc1\n"
  "v_and_b32 v5, 0x80008000, v5\n"
  "v_cmp_ne_u32 vcc, 0, v5\n"
  "v_mfma_f32_32x32x16_bf16 v[32:47], a[36:39], v[112:115], v[32:47]\n"
  "s_cbranch_vccnz .Ld0_1_f\n"
  ".Ld0_1_k:\n"
  "v_mfma_f32_32x32x16_bf16 a[224:239], a[8:11], v[116:119], a[224:239]\n"
  "s_add_u32 s62, s57, 1\n"
  "s_and_b32 s62, s62, 3\n"
  "s_mul_i32 s62, s62, 7168\n"
  "s_add_u32 s62, s62, s92\n"
  "buffer_load_dwordx4 v[184:187], v2, s[12:15], s62 offen offset:2048 sc1\n"
  "s_waitcnt vmcnt(9)\n"
  "v_mfma_f32_32x32x16_bf16 a[240:255], a[12:15], v[116:119], a[240:255]\n"
  "v_xor_b32 v5, s50, v136\n"
  "v_bitop3_b32 v5, v137, s51, v5 bitop3:0xbe\n"
  "v_bitop3_b32 v5, v138, s93, v5 bitop3:0xbe\n"
  "v_bitop3_b32 v5, v139, s101, v5 bitop3:0xbe\n"
  "v_mfma_f32_32x32x16_bf16 a[224:239], a[40:43], v[120:123], a[224:239]\n"
  "buffer_load_dwordx4 v[188:191], v2, s[12:15], s62 offen offset:3072 sc1\n"
  "v_bitop3_b32 v5, v140, s50, v5 bitop3:0xbe\n"
  "v_bitop3_b32 v5, v141, s51, v5 bitop3:0xbe\n"
  "v_mfma_f32_32x32x16_bf16 a[240:255], a[44:47], v[120:123], a[240:255]\n"
  "v_bitop3_b32 v5, v142, s93, v5 bitop3:0xbe\n"
  "v_bitop3_b32 v5, v143, s101, v5 bitop3:0xbe\n"
  "v_bitop3_b32 v5, v144, s50, v5 bitop3:0xbe\n"
  "v_bitop3_b32 v5, v145, s51, v5 bitop3:0xbe\n"
  "v_mfma_f32_32x32x16_bf16 v[16:31], a[8:11], v[128:131], v[16:31]\n"
  "buffer_load_dwordx4 v[192:195], v11, s[12:15], s62 offen offset:2048 sc1\n"
  "v_bitop3_b32 v5, v146, s93, v5 bitop3:0xbe\n"
  "v_bitop3_b32 v5, v147, s101, v5 bitop3:0xbe\n"
  "v_mfma_f32_32x32x16_bf16 v[32:47], a[12:15], v[128:131], v[32:47]\n"
  "v_bitop3_b32 v5, v148, s50, v5 bitop3:0xbe\n"
  "v_bitop3_b32 v5, v149, s51, v5 bitop3:0xbe\n"
  "v_bitop3_b32 v5, v150, s93, v5 bitop3:0xbe\n"
  "v_bitop3_b32 v5, v151, s101, v5 bitop3:0xbe\n"
  "v_mfma_f32_32x32x16_bf16 v[16:31], a[40:43], v[132:135], v[16:31]\n"
  "buffer_load_dwordx4 v[196:199], v11, s[12:15], s62 offen offset:3072 sc1\n"
  "v_and_b32 v5, 0x80008000, v5\n"
  "v_cmp_ne_u32 vcc, 0, v5\n"
  "v_mfma_f32_32x32x16_bf16 v[32:47], a[44:47], v[132:135], v[32:47]\n"
  "s_cbranch_vccnz .Ld0_2_f\n"
  ".Ld0_2_k:\n"
  "v_mfma_f32_32x32x16_bf16 a[224:239], a[16:19], v[136:139], a[224:239]\n"
  "s_add_u32 s62, s57, 2\n"
  "s_and_b32 s62, s62, 3\n"
  "s_mul_i32 s62, s62, 7168\n"
  "s_add_u32 s62, s62, s92\n"
  "buffer_load_dwordx4 v[200:203], v2, s[12:15], s62 offen offset:2048 sc1\n"
  "s_waitcnt vmcnt(9)\n"
  "v_mfma_f32_32x32x16_bf16 a[240:255], a[20:23], v[136:139], a[240:255]\n"
  "v_xor_b32 v5, s50, v152\n"
  "v_bitop3_b32 v5, v153, s51, v5 bitop3:0xbe\n"
  "v_bitop3_b32 v5, v154, s93, v5 bitop3:0xbe\n"
  "v_bitop3_b32 v5, v155, s101, v5 bitop3:0xbe\n"
  "v_mfma_f32_32x32x16_bf16 a[224:239], a[48:51], v[140:143], a[224:239]\n"
  "buffer_load_dwordx4 v[204:207], v2, s[12:15], s62 offen offset:3072 sc1\n"
  "v_bitop3_b32 v5, v156, s50, v5 bitop3:0xbe\n"
  "v_bitop3_b32 v5, v157, s51, v5 bitop3:0xbe\n"
  "v_mfma_f32_32x32x16_bf16 a[240:255], a[52:55], v[140:143], a[240:255]\n"
  "v_bitop3_b32 v5, v158, s93, v5 bitop3:0xbe\n"
  "v_bitop3_b32 v5, v159, s101, v5 bitop3:0xbe\n"
  "v_bitop3_b32 v5, v160, s50, v5 bitop3:0xbe\n"
  "v_bitop3_b32 v5, v161, s51, v5 bitop3:0xbe\n"
  "v_mfma_f32_32x32x16_bf16 v[16:31], a[16:19], v[144:147], v[16:31]\n"
  "buffer_load_dwordx4 v[208:211], v11, s[12:15], s62 offen offset:2048 sc1\n"
  "v_bitop3_b32 v5, v162, s93, v5 bitop3:0xbe\n"
  "v_bitop3_b32 v5, v163, s101, v5 bitop3:0xbe\n"
  "v_mfma_f32_32x32x16_bf16 v[32:47], a[20:23], v[144:147], v[32:47]\n"
  "v_bitop3_b32 v5, v164, s50, v5 bitop3:0xbe\n"
  "v_bitop3_b32 v5, v165, s51, v5 bitop3:0xbe\n"
  "v_bitop3_b32 v5, v166, s93, v5 bitop3:0xbe\n"
  "v_bitop3_b32 v5, v167, s101, v5 bitop3:0xbe\n"
  "v_mfma_f32_32x32x16_bf16 v[16:31], a[48:51], v[148:151], v[16:31]\n"
  "buffer_load_dwordx4 v[212:215], v11, s[12:15], s62 offen offset:3072 sc1\n"
  "v_and_b32 v5, 0x80008000, v5\n"
  "v_cmp_ne_u32 vcc, 0, v5\n"
  "v_mfma_f32_32x32x16_bf16 v[32:47], a[52:55], v[148:151], v[32:47]\n"
  "s_cbranch_vccnz .Ld0_3_f\n"
  ".Ld0_3_k:\n"
  "v_mfma_f32_32x32x16_bf16 a[224:239], a[24:27], v[152:155], a[224:239]\n"
  "s_add_u32 s62, s57, 3\n"
  "s_and_b32 s62, s62, 3\n"
  "s_mul_i32 s62, s62, 7168\n"
  "s_add_u32 s62, s62, s92\n"
  "buffer_load_dwordx4 v[216:219], v2, s[12:15], s62 offen offset:2048 sc1\n"
  "s_waitcnt vmcnt(9)\n"
  "v_mfma_f32_32x32x16_bf16 a[240:255], a[28:31], v[152:155], a[240:255]\n"
  "v_xor_b32 v5, s50, v168\n"
  "v_bitop3_b32 v5, v169, s51, v5 bitop3:0xbe\n"
  "v_bitop3_b32 v5, v170, s93, v5 bitop3:0xbe\n"
  "v_bitop3_b32 v5, v171, s101, v5 bitop3:0xbe\n"
  "v_mfma_f32_32x32x16_bf16 a[224:239], a[56:59], v[156:159], a[224:239]\n"
  "buffer_load_dwordx4 v[220:223], v2, s[12:15], s62 offen offset:3072 sc1\n"
  "v_bitop3_b32 v5, v172, s50, v5 bitop3:0xbe\n"
  "v_bitop3_b32 v5, v173, s51, v5 bitop3:0xbe\n"
  "v_mfma_f32_32x32x16_bf16 a[240:255], a[60:63], v[156:159], a[240:255]\n"
  "v_bitop3_b32 v5, v174, s93, v5 bitop3:0xbe\n"
  "v_bitop3_b32 v5, v175, s101, v5 bitop3:0xbe\n"
  "v_bitop3_b32 v5, v176, s50, v5 bitop3:0xbe\n"
  "v_bitop3_b32 v5, v177, s51, v5 bitop3:0xbe\n"
  "v_mfma_f32_32x32x16_bf16 v[16:31], a[24:27], v[160:163], v[16:31]\n"
  "buffer_load_dwordx4 v[224:227], v11, s[12:15], s62 offen offset:2048 sc1\n"
  "v_bitop3_b32 v5, v178, s93, v5 bitop3:0xbe\n"
  "v_bitop3_b32 v5, v179, s101, v5 bitop3:0xbe\n"
  "v_mfma_f32_32x32x16_bf16 v[32:47], a[28:31], v[160:163], v[32:47]\n"
  "v_bitop3_b32 v5, v180, s50, v5 bitop3:0xbe\n"
  "v_bitop3_b32 v5, v181, s51, v5 bitop3:0xbe\n"
  "v_bitop3_b32 v5, v182, s93, v5 bitop3:0xbe\n"
  "v_bitop3_b32 v5, v183, s101, v5 bitop3:0xbe\n"
  "v_mfma_f32_32x32x16_bf16 v[16:31], a[56:59], v[164:167], v[16:31]\n"
  "buffer_load_dwordx4 v[228:231], v11, s[12:15], s62 offen offset:3072 sc1\n"
  "v_and_b32 v5, 0x80008000, v5\n"
  "v_cmp_ne_u32 vcc, 0, v5\n"
  "v_mfma_f32_32x32x16_bf16 v[32:47], a[60:63], v[164:167], v[32:47]\n"
  "s_cbranch_vccnz .Ld1_0_f\n"
  ".Ld1_0_k:\n"
  "v_mfma_f32_32x32x16_bf16 a[224:239], a[64:67], v[168:171], a[224:239]\n"
  "s_add_u32 s62, s57, 0\n"
  "s_and_b32 s62, s62, 3\n"
  "s_mul_i32 s62, s62, 7168\n"
  "s_add_u32 s62, s62, s92\n"
  "s_add_u32 s62, s62, 4096\n"
  "buffer_load_dwordx4 v[232:235], v2, s[12:15], s62 offen offset:0 sc1\n"
  "s_waitcnt vmcnt(9)\n"
  "v_mfma_f32_32x32x16_bf16 a[240:255], a[68:71], v[168:171], a[240:255]\n"
  "v_xor_b32 v5, s50, v184\n"
  "v_bitop3_b32 v5, v185, s51, v5 bitop3:0xbe\n"
  "v_bitop3_b32 v5, v186, s93, v5 bitop3:0xbe\n"
  "v_bitop3_b32 v5, v187, s101, v5 bitop3:0xbe\n"
  "v_mfma_f32_32x32x16_bf16 a[224:239], a[96:99], v[172:175], a[224:239]\n"
  "buffer_load_dwordx4 v[236:239], v2, s[12:15], s62 offen offset:1024 sc1\n"
  "v_bitop3_b32 v5, v188, s50, v5 bitop3:0xbe\n"
  "v_bitop3_b32 v5, v189, s51, v5 bitop3:0xbe\n"
  "v_mfma_f32_32x32x16_bf16 a[240:255], a[100:103], v[172:175], a[240:255]\n"
  "v_bitop3_b32 v5, v190, s93, v5 bitop3:0xbe\n"
  "v_bitop3_b32 v5, v191, s101, v5 bitop3:0xbe\n"
  "v_bitop3_b32 v5, v192, s50, v5 bitop3:0xbe\n"
  "v_bitop3_b32 v5, v193, s51, v5 bitop3:0xbe\n"
  "v_mfma_f32_32x32x16_bf16 v[16:31], a[64:67], v[176:179], v[16:31]\n"
  "buffer_load_dwordx4 v[64:67], v11, s[12:15], s62 offen offset:0 sc1\n"
  "v_bitop3_b32 v5, v194, s93, v5 bitop3:0xbe\n"
  "v_bitop3_b32 v5, v195, s101, v5 bitop3:0xbe\n"
  "v_mfma_f32_32x32x16_bf16 v[32:47], a[68:71], v[176:179], v[32:47]\n"
  "v_bitop3_b32 v5, v196, s50, v5 bitop3:0xbe\n"
  "v_bitop3_b32 v5, v197, s51, v5 bitop3:0xbe\n"
  "v_bitop3_b32 v5, v198, s93, v5 bitop3:0xbe\n"
  "v_bitop3_b32 v5, v199, s101, v5 bitop3:0xbe\n"
  "v_mfma_f32_32x32x16_bf16 v[16:31], a[96:99], v[180:183], v[16:31]\n"
  "buffer_load_dwordx4 v[68:71], v11, s[12:15], s62 offen offset:1024 sc1\n"
  "v_and_b32 v5, 0x80008000, v5\n"
  "v_cmp_ne_u32 vcc, 0, v5\n"
  "v_mfma_f32_32x32x16_bf16 v[32:47], a[100:103], v[180:183], v[32:47]\n"
  "s_cbranch_vccnz .Ld1_1_f\n"
  ".Ld1_1_k:\n"
  "v_mfma_f32_32x32x16_bf16 a[224:239], a[72:75], v[184:187], a[224:239]\n"
  "s_add_u32 s62, s57, 1\n"
  "s_and_b32 s62, s62, 3\n"
  "s_mul_i32 s62, s62, 7168\n"
  "s_add_u32 s62, s62, s92\n"
  "s_add_u32 s62, s62, 4096\n"
  "buffer_load_dwordx4 v[72:75], v2, s[12:15], s62 offen offset:0 sc1\n"
  "s_waitcnt vmcnt(9)\n"
  "v_mfma_f32_32x32x16_bf16 a[240:255], a[76:79], v[184:187], a[240:255]\n"
  "v_xor_b32 v5, s50, v200\n"
  "v_bitop3_b32 v5, v201, s51, v5 bitop3:0xbe\n"
  "v_bitop3_b32 v5, v202, s93, v5 bitop3:0xbe\n"
  "v_bitop3_b32 v5, v203, s101, v5 bitop3:0xbe\n"
  "v_mfma_f32_32x32x16_bf16 a[224:239], a[104:107], v[188:191], a[224:239]\n"
  "buffer_load_dwordx4 v[76:79], v2, s[12:15], s62 offen offset:1024 sc1\n"
  "v_bitop3_b32 v5, v204, s50, v5 bitop3:0xbe\n"
  "v_bitop3_b32 v5, v205, s51, v5 bitop3:0xbe\n"
  "v_mfma_f32_32x32x16_bf16 a[240:255], a[108:111], v[188:191], a[240:255]\n"
  "v_bitop3_b32 v5, v206, s93, v5 bitop3:0xbe\n"
  "v_bitop3_b32 v5, v207, s101, v5 bitop3:0xbe\n"
  "v_bitop3_b32 v5, v208, s50, v5 bitop3:0xbe\n"
  "v_bitop3_b32 v5, v209, s51, v5 bitop3:0xbe\n"
  "v_mfma_f32_32x32x16_bf16 v[16:31], a[72:75], v[192:195], v[16:31]\n"
  "buffer_load_dwordx4 v[80:83], v11, s[12:15], s62 offen offset:0 sc1\n"
  "v_bitop3_b32 v5, v210, s93, v5 bitop3:0xbe\n"
  "v_bitop3_b32 v5, v211, s101, v5 bitop3:0xbe\n"
  "v_mfma_f32_32x32x16_bf16 v[32:47], a[76:79], v[192:195], v[32:47]\n"
  "v_bitop3_b32 v5, v212, s50, v5 bitop3:0xbe\n"
  "v_bitop3_b32 v5, v213, s51, v5 bitop3:0xbe\n"
  "v_bitop3_b32 v5, v214, s93, v5 bitop3:0xbe\n"
  "v_bitop3_b32 v5, v215, s101, v5 bitop3:0xbe\n"
  "v_mfma_f32_32x32x16_bf16 v[16:31], a[104:107], v[196:199], v[16:31]\n"
  "buffer_load_dwordx4 v[84:87], v11, s[12:15], s62 offen offset:1024 sc1\n"
  "v_and_b32 v5, 0x80008000, v5\n"
  "v_cmp_ne_u32 vcc, 0, v5\n"
  "v_mfma_f32_32x32x16_bf16 v[32:47], a[108:111], v[196:199], v[32:47]\n"
  "s_cbranch_vccnz .Ld1_2_f\n"
  ".Ld1_2_k:\n"
  "v_mfma_f32_32x32x16_bf16 a[224:239], a[80:83], v[200:203], a[224:239]\n"
  "s_add_u32 s62, s57, 2\n"
  "s_and_b32 s62, s62, 3\n"
  "s_mul_i32 s62, s62, 7168\n"
  "s_add_u32 s62, s62, s92\n"
  "s_add_u32 s62, s62, 4096\n"
  "buffer_load_dwordx4 v[88:91], v2, s[12:15], s62 offen offset:0 sc1\n"
  "s_waitcnt vmcnt(9)\n"
  "v_mfma_f32_32x32x16_bf16 a[240:255], a[84:87], v[200:203], a[240:255]\n"
  "v_xor_b32 v5, s50, v216\n"
  "v_bitop3_b32 v5, v217, s51, v5 bitop3:0xbe\n"
  "v_bitop3_b32 v5, v218, s93, v5 bitop3:0xbe\n"
  "v_bitop3_b32 v5, v219, s101, v5 bitop3:0xbe\n"
  "v_mfma_f32_32x32x16_bf16 a[224:239], a[112:115], v[204:207], a[224:239]\n"
  "buffer_load_dwordx4 v[92:95], v2, s[12:15], s62 offen offset:1024 sc1\n"
  "v_bitop3_b32 v5, v220, s50, v5 bitop3:0xbe\n"
  "v_bitop3_b32 v5, v221, s51, v5 bitop3:0xbe\n"
  "v_mfma_f32_32x32x16_bf16 a[240:255], a[116:119], v[204:207], a[240:255]\n"
  "v_bitop3_b32 v5, v222, s93, v5 bitop3:0xbe\n"
  "v_bitop3_b32 v5, v223, s101, v5 bitop3:0xbe\n"
  "v_bitop3_b32 v5, v224, s50, v5 bitop3:0xbe\n"
  "v_bitop3_b32 v5, v225, s51, v5 bitop3:0xbe\n"
  "v_mfma_f32_32x32x16_bf16 v[16:31], a[80:83], v[208:211], v[16:31]\n"
  "buffer_load_dwordx4 v[48:51], v11, s[12:15], s62 offen offset:0 sc1\n"
  "v_bitop3_b32 v5, v226, s93, v5 bitop3:0xbe\n"
  "v_bitop3_b32 v5, v227, s101, v5 bitop3:0xbe\n"
  "v_mfma_f32_32x32x16_bf16 v[32:47], a[84:87], v[208:211], v[32:47]\n"
  "v_bitop3_b32 v5, v228, s50, v5 bitop3:0xbe\n"
  "v_bitop3_b32 v5, v229, s51, v5 bitop3:0xbe\n"
  "v_bitop3_b32 v5, v230, s93, v5 bitop3:0xbe\n"
  "v_bitop3_b32 v5, v231, s101, v5 bitop3:0xbe\n"
  "v_mfma_f32_32x32x16_bf16 v[16:31], a[112:115], v[212:215], v[16:31]\n"
  "buffer_load_dwordx4 v[52:55], v11, s[12:15], s62 offen offset:1024 sc1\n"
  "v_and_b32 v5, 0x80008000, v5\n"
  "v_cmp_ne_u32 vcc, 0, v5\n"
  "v_mfma_f32_32x32x16_bf16 v[32:47], a[116:119], v[212:215], v[32:47]\n"
  "s_cbranch_vccnz .Ld1_3_f\n"
  ".Ld1_3_k:\n"
  "v_mfma_f32_32x32x16_bf16 a[224:239], a[88:91], v[216:219], a[224:239]\n"
  "s_add_u32 s62, s57, 3\n"
  "s_and_b32 s62, s62, 3\n"
  "s_mul_i32 s62, s62, 7168\n"
  "s_add_u32 s62, s62, s92\n"
  "s_add_u32 s62, s62, 4096\n"
  "buffer_load_dwordx4 v[56:59], v2, s[12:15], s62 offen offset:0 sc1\n"
  "s_waitcnt vmcnt(9)\n"
  "v_mfma_f32_32x32x16_bf16 a[240:255], a[92:95], v[216:219], a[240:255]\n"
  "v_xor_b32 v5, s50, v232\n"
  "v_bitop3_b32 v5, v233, s51, v5 bitop3:0xbe\n"
  "v_bitop3_b32 v5, v234, s93, v5 bitop3:0xbe\n"
  "v_bitop3_b32 v5, v235, s101, v5 bitop3:0xbe\n"
  "v_mfma_f32_32x32x16_bf16 a[224:239], a[120:123], v[220:223], a[224:239]\n"
  "buffer_load_dwordx4 v[60:63], v2, s[12:15], s62 offen offset:1024 sc1\n"
  "v_bitop3_b32 v5, v236, s50, v5 bitop3:0xbe\n"
  "v_bitop3_b32 v5, v237, s51, v5 bitop3:0xbe\n"
  "v_mfma_f32_32x32x16_bf16 a[240:255], a[124:127], v[220:223], a[240:255]\n"
  "v_bitop3_b32 v5, v238, s93, v5 bitop3:0xbe\n"
  "v_bitop3_b32 v5, v239, s101, v5 bitop3:0xbe\n"
  "v_bitop3_b32 v5, v64, s50, v5 bitop3:0xbe\n"
  "v_bitop3_b32 v5, v65, s51, v5 bitop3:0xbe\n"
  "v_mfma_f32_32x32x16_bf16 v[16:31], a[88:91], v[224:227], v[16:31]\n"
  "buffer_load_dwordx4 v[240:243], v11, s[12:15], s62 offen offset:0 sc1\n"
  "v_bitop3_b32 v5, v66, s93, v5 bitop3:0xbe\n"
  "v_bitop3_b32 v5, v67, s101, v5 bitop3:0xbe\n"
  "v_mfma_f32_32x32x16_bf16 v[32:47], a[92:95], v[224:227], v[32:47]\n"
  "v_bitop3_b32 v5, v68, s50, v5 bitop3:0xbe\n"
  "v_bitop3_b32 v5, v69, s51, v5 bitop3:0xbe\n"
  "v_bitop3_b32 v5, v70, s93, v5 bitop3:0xbe\n"
  "v_bitop3_b32 v5, v71, s101, v5 bitop3:0xbe\n"
  "v_mfma_f32_32x32x16_bf16 v[16:31], a[120:123], v[228:231], v[16:31]\n"
  "buffer_load_dwordx4 v[244:247], v11, s[12:15], s62 offen offset:1024 sc1\n"
  "v_and_b32 v5, 0x80008000, v5\n"
  "v_cmp_ne_u32 vcc, 0, v5\n"
  "v_mfma_f32_32x32x16_bf16 v[32:47], a[124:127], v[228:231], v[32:47]\n"
  "s_cbranch_vccnz .Ld2_0_f\n"
  ".Ld2_0_k:\n"
  "v_mfma_f32_32x32x16_bf16 a[224:239], a[128:131], v[232:235], a[224:239]\n"
  "s_add_u32 s62, s57, 0\n"
  "s_and_b32 s62, s62, 3\n"
  "s_mul_i32 s62, s62, 7168\n"
  "s_add_u32 s62, s62, s92\n"
  "s_add_u32 s62, s62, 4096\n"
  "buffer_load_dwordx4 v[248:251], v2, s[12:15], s62 offen offset:2048 sc1\n"
  "s_waitcnt vmcnt(9)\n"
  "v_mfma_f32_32x32x16_bf16 a[240:255], a[132:135], v[232:235], a[240:255]\n"
  "v_xor_b32 v5, s50, v72\n"
  "v_bitop3_b32 v5, v73, s51, v5 bitop3:0xbe\n"
  "v_bitop3_b32 v5, v74, s93, v5 bitop3:0xbe\n"
  "v_bitop3_b32 v5, v75, s101, v5 bitop3:0xbe\n"
  "v_mfma_f32_32x32x16_bf16 a[224:239], a[160:163], v[236:239], a[224:239]\n"
  "s_add_u32 s62, s57, 1\n"
  "s_and_b32 s62, s62, 3\n"
  "s_mul_i32 s62, s62, 7168\n"
  "s_add_u32 s62, s62, s92\n"
  "s_add_u32 s62, s62, 4096\n"
  "buffer_load_dwordx4 v[112:115], v2, s[12:15], s62 offen offset:2048 sc1\n"
  "v_bitop3_b32 v5, v76, s50, v5 bitop3:0xbe\n"
  "v_bitop3_b32 v5, v77, s51, v5 bitop3:0xbe\n"
  "v_mfma_f32_32x32x16_bf16 a[240:255], a[164:167], v[236:239], a[240:255]\n"
  "v_bitop3_b32 v5, v78, s93, v5 bitop3:0xbe\n"
  "v_bitop3_b32 v5, v79, s101, v5 bitop3:0xbe\n"
  "v_bitop3_b32 v5, v80, s50, v5 bitop3:0xbe\n"
  "v_bitop3_b32 v5, v81, s51, v5 bitop3:0xbe\n"
  "v_mfma_f32_32x32x16_bf16 v[16:31], a[128:131], v[64:67], v[16:31]\n"
  "s_add_u32 s62, s57, 2\n"
  "s_and_b32 s62, s62, 3\n"
  "s_mul_i32 s62, s62, 7168\n"
  "s_add_u32 s62, s62, s92\n"
  "s_add_u32 s62, s62, 4096\n"
  "buffer_load_dwordx4 v[116:119], v2, s[12:15], s62 offen offset:2048 sc1\n"
  "v_bitop3_b32 v5, v82, s93, v5 bitop3:0xbe\n"
  "v_bitop3_b32 v5, v83, s101, v5 bitop3:0xbe\n"
  "v_mfma_f32_32x32x16_bf16 v[32:47], a[132:135], v[64:67], v[32:47]\n"
  "v_bitop3_b32 v5, v84, s50, v5 bitop3:0xbe\n"
  "v_bitop3_b32 v5, v85, s51, v5 bitop3:0xbe\n"
  "v_bitop3_b32 v5, v86, s93, v5 bitop3:0xbe\n"
  "v_bitop3_b32 v5, v87, s101, v5 bitop3:0xbe\n"
  "v_mfma_f32_32x32x16_bf16 v[16:31], a[160:163], v[68:71], v[16:31]\n"
  "s_add_u32 s62, s57, 3\n"
  "s_and_b32 s62, s62, 3\n"
  "s_mul_i32 s62, s62, 7168\n"
  "s_add_u32 s62, s62, s92\n"
  "s_add_u32 s62, s62, 4096\n"
  "buffer_load_dwordx4 v[120:123], v2, s[12:15], s62 offen offset:2048 sc1\n"
  "v_and_b32 v5, 0x80008000, v5\n"
  "v_cmp_ne_u32 vcc, 0, v5\n"
  "v_mfma_f32_32x32x16_bf16 v[32:47], a[164:167], v[68:71], v[32:47]\n"
  "s_cbranch_vccnz .Ld2_1_f\n"
  ".Ld2_1_k:\n"
  "v_mfma_f32_32x32x16_bf16 a[224:239], a[136:139], v[72:75], a[224:239]\n"
  "s_add_u32 s62, s57, 0\n"
  "s_and_b32 s62, s62, 3\n"
  "s_mul_i32 s62, s62, 7168\n"
  "s_add_u32 s62, s62, s92\n"
  "s_add_u32 s62, s62, 4096\n"
  "buffer_load_dwordx4 v[128:131], v11, s[12:15], s62 offen offset:2048 sc1\n"
  "s_waitcnt vmcnt(9)\n"
  "v_mfma_f32_32x32x16_bf16 a[240:255], a[140:143], v[72:75], a[240:255]\n"
  "v_xor_b32 v5, s50, v88\n"
  "v_bitop3_b32 v5, v89, s51, v5 bitop3:0xbe\n"
  "v_bitop3_b32 v5, v90, s93, v5 bitop3:0xbe\n"
  "v_bitop3_b32 v5, v91, s101, v5 bitop3:0xbe\n"
  "v_mfma_f32_32x32x16_bf16 a[224:239], a[168:171], v[76:79], a[224:239]\n"
  "s_add_u32 s62, s57, 1\n"
  "s_and_b32 s62, s62, 3\n"
  "s_mul_i32 s62, s62, 7168\n"
  "s_add_u32 s62, s62, s92\n"
  "s_add_u32 s62, s62, 4096\n"
  "buffer_load_dwordx4 v[132:135], v11, s[12:15], s62 offen offset:2048 sc1\n"
  "v_bitop3_b32 v5, v92, s50, v5 bitop3:0xbe\n"
  "v_bitop3_b32 v5, v93, s51, v5 bitop3:0xbe\n"
  "v_mfma_f32_32x32x16_bf16 a[240:255], a[172:175], v[76:79], a[240:255]\n"
  "v_bitop3_b32 v5, v94, s93, v5 bitop3:0xbe\n"
  "v_bitop3_b32 v5, v95, s101, v5 bitop3:0xbe\n"
  "v_bitop3_b32 v5, v48, s50, v5 bitop3:0xbe\n"
  "v_bitop3_b32 v5, v49, s51, v5 bitop3:0xbe\n"
  "v_mfma_f32_32x32x16_bf16 v[16:31], a[136:139], v[80:83], v[16:31]\n"
  "s_add_u32 s62, s57, 2\n"
  "s_and_b32 s62, s62, 3\n"
  "s_mul_i32 s62, s62, 7168\n"
  "s_add_u32 s62, s62, s92\n"
  "s_add_u32 s62, s62, 4096\n"
  "buffer_load_dwordx4 v[136:139], v11, s[12:15], s62 offen offset:2048 sc1\n"
  "v_bitop3_b32 v5, v50, s93, v5 bitop3:0xbe\n"
  "v_bitop3_b32 v5, v51, s101, v5 bitop3:0xbe\n"
  "v_mfma_f32_32x32x16_bf16 v[32:47], a[140:143], v[80:83], v[32:47]\n"
  "v_bitop3_b32 v5, v52, s50, v5 bitop3:0xbe\n"
  "v_bitop3_b32 v5, v53, s51, v5 bitop3:0xbe\n"
  "v_bitop3_b32 v5, v54, s93, v5 bitop3:0xbe\n"
  "v_bitop3_b32 v5, v55, s101, v5 bitop3:0xbe\n"
  "v_mfma_f32_32x32x16_bf16 v[16:31], a[168:171], v[84:87], v[16:31]\n"
  "s_add_u32 s62, s57, 3\n"
  "s_and_b32 s62, s62, 3\n"
  "s_mul_i32 s62, s62, 7168\n"
  "s_add_u32 s62, s62, s92\n"
  "s_add_u32 s62, s62, 4096\n"
  "buffer_load_dwordx4 v[140:143], v11, s[12:15], s62 offen offset:2048 sc1\n"
  "v_and_b32 v5, 0x80008000, v5\n"
  "v_cmp_ne_u32 vcc, 0, v5\n"
  "v_mfma_f32_32x32x16_bf16 v[32:47], a[172:175], v[84:87], v[32:47]\n"
  "s_cbranch_vccnz .Ld2_2_f\n"
  ".Ld2_2_k:\n"
  "v_mfma_f32_32x32x16_bf16 a[224:239], a[144:147], v[88:91], a[224:239]\n"
  "s_waitcnt vmcnt(8)\n"
  "v_mfma_f32_32x32x16_bf16 a[240:255], a[148:151], v[88:91], a[240:255]\n"
  "v_xor_b32 v5, s50, v56\n"
  "v_bitop3_b32 v5, v57, s51, v5 bitop3:0xbe\n"
  "v_bitop3_b32 v5, v58, s93, v5 bitop3:0xbe\n"
  "v_bitop3_b32 v5, v59, s101, v5 bitop3:0xbe\n"
  "v_mfma_f32_32x32x16_bf16 a[224:239], a[176:179], v[92:95], a[224:239]\n"
  "v_bitop3_b32 v5, v60, s50, v5 bitop3:0xbe\n"
  "v_bitop3_b32 v5, v61, s51, v5 bitop3:0xbe\n"
  "v_mfma_f32_32x32x16_bf16 a[240:255], a[180:183], v[92:95], a[240:255]\n"
  "v_bitop3_b32 v5, v62, s93, v5 bitop3:0xbe\n"
  "v_bitop3_b32 v5, v63, s101, v5 bitop3:0xbe\n"
  "v_bitop3_b32 v5, v240, s50, v5 bitop3:0xbe\n"
  "v_bitop3_b32 v5, v241, s51, v5 bitop3:0xbe\n"
  "v_mfma_f32_32x32x16_bf16 v[16:31], a[144:147], v[48:51], v[16:31]\n"
  "v_bitop3_b32 v5, v242, s93, v5 bitop3:0xbe\n"
  "v_bitop3_b32 v5, v243, s101, v5 bitop3:0xbe\n"
  "v_mfma_f32_32x32x16_bf16 v[32:47], a[148:151], v[48:51], v[32:47]\n"
  "v_bitop3_b32 v5, v244, s50, v5 bitop3:0xbe\n"
  "v_bitop3_b32 v5, v245, s51, v5 bitop3:0xbe\n"
  "v_bitop3_b32 v5, v246, s93, v5 bitop3:0xbe\n"
  "v_bitop3_b32 v5, v247, s101, v5 bitop3:0xbe\n"
  "v_mfma_f32_32x32x16_bf16 v[16:31], a[176:179], v[52:55], v[16:31]\n"
  "v_and_b32 v5, 0x80008000, v5\n"
  "v_cmp_ne_u32 vcc, 0, v5\n"
  "v_mfma_f32_32x32x16_bf16 v[32:47], a[180:183], v[52:55], v[32:47]\n"
  "s_cbranch_vccnz .Ld2_3_f\n"
  ".Ld2_3_k:\n"
  "v_mfma_f32_32x32x16_bf16 a[224:239], a[152:155], v[56:59], a[224:239]\n"
  "s_waitcnt vmcnt(4)\n"
  "v_mfma_f32_32x32x16_bf16 a[240:255], a[156:159], v[56:59], a[240:255]\n"
  "v_xor_b32 v5, s50, v248\n"
  "v_bitop3_b32 v5, v249, s51, v5 bitop3:0xbe\n"
  "v_bitop3_b32 v5, v250, s93, v5 bitop3:0xbe\n"
  "v_bitop3_b32 v5, v251, s101, v5 bitop3:0xbe\n"
  "v_mfma_f32_32x32x16_bf16 a[224:239], a[184:187], v[60:63], a[224:239]\n"
  "v_bitop3_b32 v5, v112, s50, v5 bitop3:0xbe\n"
  "v_bitop3_b32 v5, v113, s51, v5 bitop3:0xbe\n"
  "v_mfma_f32_32x32x16_bf16 a[240:255], a[188:191], v[60:63], a[240:255]\n"
  "v_bitop3_b32 v5, v114, s93, v5 bitop3:0xbe\n"
  "v_bitop3_b32 v5, v115, s101, v5 bitop3:0xbe\n"
  "v_bitop3_b32 v5, v116, s50, v5 bitop3:0xbe\n"
  "v_bitop3_b32 v5, v117, s51, v5 bitop3:0xbe\n"
  "v_mfma_f32_32x32x16_bf16 v[16:31], a[152:155], v[240:243], v[16:31]\n"
  "v_bitop3_b32 v5, v118, s93, v5 bitop3:0xbe\n"
  "v_bitop3_b32 v5, v119, s101, v5 bitop3:0xbe\n"
  "v_mfma_f32_32x32x16_bf16 v[32:47], a[156:159], v[240:243], v[32:47]\n"
  "v_bitop3_b32 v5, v120, s50, v5 bitop3:0xbe\n"
  "v_bitop3_b32 v5, v121, s51, v5 bitop3:0xbe\n"
  "v_bitop3_b32 v5, v122, s93, v5 bitop3:0xbe\n"
  "v_bitop3_b32 v5, v123, s101, v5 bitop3:0xbe\n"
  "v_mfma_f32_32x32x16_bf16 v[16:31], a[184:187], v[244:247], v[16:31]\n"
  "v_and_b32 v5, 0x80008000, v5\n"
  "v_cmp_ne_u32 vcc, 0, v5\n"
  "v_mfma_f32_32x32x16_bf16 v[32:47], a[188:191], v[244:247], v[32:47]\n"
  "s_cbranch_vccnz .Ld3_0_f\n"
  ".Ld3_0_k:\n"
  "v_mfma_f32_32x32x16_bf16 a[224:239], a[192:195], v[248:251], a[224:239]\n"
  "s_waitcnt vmcnt(0)\n"
  "v_mfma_f32_32x32x16_bf16 a[240:255], a[196:199], v[248:251], a[240:255]\n"
  "v_xor_b32 v5, s50, v128\n"
  "v_bitop3_b32 v5, v129, s51, v5 bitop3:0xbe\n"
  "v_bitop3_b32 v5, v130, s93, v5 bitop3:0xbe\n"
  "v_bitop3_b32 v5, v131, s101, v5 bitop3:0xbe\n"
  "v_mfma_f32_32x32x16_bf16 a[224:239], a[200:203], v[112:115], a[224:239]\n"
  "v_bitop3_b32 v5, v132, s50, v5 bitop3:0xbe\n"
  "v_bitop3_b32 v5, v133, s51, v5 bitop3:0xbe\n"
  "v_mfma_f32_32x32x16_bf16 a[240:255], a[204:207], v[112:115], a[240:255]\n"
  "v_bitop3_b32 v5, v134, s93, v5 bitop3:0xbe\n"
  "v_bitop3_b32 v5, v135, s101, v5 bitop3:0xbe\n"
  "v_bitop3_b32 v5, v136, s50, v5 bitop3:0xbe\n"
  "v_bitop3_b32 v5, v137, s51, v5 bitop3:0xbe\n"
  "v_mfma_f32_32x32x16_bf16 a[224:239], a[208:211], v[116:119], a[224:239]\n"
  "v_bitop3_b32 v5, v138, s93, v5 bitop3:0xbe\n"
  "v_bitop3_b32 v5, v139, s101, v5 bitop3:0xbe\n"
  "v_mfma_f32_32x32x16_bf16 a[240:255], a[212:215], v[116:119], a[240:255]\n"
  "v_bitop3_b32 v5, v140, s50, v5 bitop3:0xbe\n"
  "v_bitop3_b32 v5, v141, s51, v5 bitop3:0xbe\n"
  "v_bitop3_b32 v5, v142, s93, v5 bitop3:0xbe\n"
  "v_bitop3_b32 v5, v143, s101, v5 bitop3:0xbe\n"
  "v_mfma_f32_32x32x16_bf16 a[224:239], a[216:219], v[120:123], a[224:239]\n"
  "v_and_b32 v5, 0x80008000, v5\n"
  "v_cmp_ne_u32 vcc, 0, v5\n"
  "v_mfma_f32_32x32x16_bf16 a[240:255], a[220:223], v[120:123], a[240:255]\n"
  "s_cbranch_vccnz .Ld3_1_f\n"
  ".Ld3_1_k:\n"
  "v_mfma_f32_32x32x16_bf16 v[16:31], a[192:195], v[128:131], v[16:31]\n"
  "v_mfma_f32_32x32x16_bf16 v[32:47], a[196:199], v[128:131], v[32:47]\n"
  "v_mfma_f32_32x32x16_bf16 v[16:31], a[200:203], v[132:135], v[16:31]\n"
  "s_nop 4\n"
  "ds_write_b128 v124, a[232:235] offset:0\n"
  "v_mfma_f32_32x32x16_bf16 v[32:47], a[204:207], v[132:135], v[32:47]\n"
  "ds_write_b128 v124, a[236:239] offset:1024\n"
  "v_mfma_f32_32x32x16_bf16 v[16:31], a[208:211], v[136:139], v[16:31]\n"
  "ds_write_b128 v125, a[240:243] offset:0\n"
  "v_mfma_f32_32x32x16_bf16 v[32:47], a[212:215], v[136:139], v[32:47]\n"
  "ds_write_b128 v125, a[244:247] offset:1024\n"
  "v_mfma_f32_32x32x16_bf16 v[16:31], a[216:219], v[140:143], v[16:31]\n"
  "ds_write_b128 v126, a[248:251] offset:0\n"
  "v_mfma_f32_32x32x16_bf16 v[32:47], a[220:223], v[140:143], v[32:47]\n"
  "ds_write_b128 v126, a[252:255] offset:1024\n"
  "v_accvgpr_read_b32 v64, a224\n"
  "v_accvgpr_read_b32 v65, a225\n"
  "v_accvgpr_read_b32 v66, a226\n"
  "v_accvgpr_read_b32 v67, a227\n"
  "v_accvgpr_read_b32 v68, a228\n"
  "v_accvgpr_read_b32 v69, a229\n"
  "v_accvgpr_read_b32 v70, a230\n"
  "v_accvgpr_read_b32 v71, a231\n"
  "s_nop 2\n"
  "ds_write_b128 v125, v[32:35] offset:24576\n"
  "ds_write_b128 v125, v[36:39] offset:25600\n"
  "ds_write_b128 v126, v[40:43] offset:24576\n"
  "ds_write_b128 v126, v[44:47] offset:25600\n"
  "ds_write_b128 v124, v[24:27] offset:24576\n"
  "ds_write_b128 v124, v[28:31] offset:25600\n"
  "v_pk_add_f32 v[96:97], v[96:97], v[64:65]\n"
  "v_pk_add_f32 v[98:99], v[98:99], v[66:67]\n"
  "v_pk_add_f32 v[100:101], v[100:101], v[68:69]\n"
  "v_pk_add_f32 v[102:103], v[102:103], v[70:71]\n"
  "v_pk_add_f32 v[104:105], v[104:105], v[16:17]\n"
  "v_pk_add_f32 v[106:107], v[106:107], v[18:19]\n"
  "v_pk_add_f32 v[108:109], v[108:109], v[20:21]\n"
  "v_pk_add_f32 v[110:111], v[110:111], v[22:23]\n"
  "s_waitcnt lgkmcnt(0)\n"
  "s_barrier\n"
  "ds_read_b128 v[128:131], v127 offset:0\n"
  "ds_read_b128 v[132:135], v127 offset:1024\n"
  "ds_read_b128 v[136:139], v127 offset:2048\n"
  "ds_read_b128 v[140:143], v127 offset:3072\n"
  "ds_read_b128 v[144:147], v127 offset:4096\n"
  "ds_read_b128 v[148:151], v127 offset:5120\n"
  "ds_read_b128 v[152:155], v127 offset:24576\n"
  "ds_read_b128 v[156:159], v127 offset:25600\n"
  "ds_read_b128 v[160:163], v127 offset:26624\n"
  "ds_read_b128 v[164:167], v127 offset:27648\n"
  "ds_read_b128 v[168:171], v127 offset:28672\n"
  "ds_read_b128 v[172:175], v127 offset:29696\n"
  "s_add_u32 s58, s99, 28672\n"
  "s_waitcnt lgkmcnt(10)\n"
  "v_pk_add_f32 v[96:97], v[96:97], v[128:129]\n"
  "v_pk_add_f32 v[98:99], v[98:99], v[130:131]\n"
  "v_pk_add_f32 v[100:101], v[100:101], v[132:133]\n"
  "v_pk_add_f32 v[102:103], v[102:103], v[134:135]\n"
  "s_waitcnt lgkmcnt(8)\n"
  "v_pk_add_f32 v[96:97], v[96:97], v[136:137]\n"
  "v_pk_add_f32 v[98:99], v[98:99], v[138:139]\n"
  "v_pk_add_f32 v[100:101], v[100:101], v[140:141]\n"
  "v_pk_add_f32 v[102:103], v[102:103], v[142:143]\n"
  "s_waitcnt lgkmcnt(6)\n"
  "v_pk_add_f32 v[96:97], v[96:97], v[144:145]\n"
  "v_pk_add_f32 v[98:99], v[98:99], v[146:147]\n"
  "v_pk_add_f32 v[100:101], v[100:101], v[148:149]\n"
  "v_pk_add_f32 v[102:103], v[102:103], v[150:151]\n"
  "v_cvt_pk_bf16_f32 v48, v96, v97\n"
  "v_cvt_pk_bf16_f32 v49, v98, v99\n"
  "v_cvt_pk_bf16_f32 v50, v100, v101\n"
  "v_cvt_pk_bf16_f32 v51, v102, v103\n"
  "s_nop 0\n"
  "v_permlane32_swap_b32 v48, v50\n"
  "v_permlane32_swap_b32 v49, v51\n"
  "buffer_store_dwordx4 v[48:51], v15, s[40:43], s99 offen\n"
  "s_waitcnt lgkmcnt(4)\n"
  "v_pk_add_f32 v[104:105], v[104:105], v[152:153]\n"
  "v_pk_add_f32 v[106:107], v[106:107], v[154:155]\n"
  "v_pk_add_f32 v[108:109], v[108:109], v[156:157]\n"
  "v_pk_add_f32 v[110:111], v[110:111], v[158:159]\n"
  "s_waitcnt lgkmcnt(2)\n"
  "v_pk_add_f32 v[104:105], v[104:105], v[160:161]\n"
  "v_pk_add_f32 v[106:107], v[106:107], v[162:163]\n"
  "v_pk_add_f32 v[108:109], v[108:109], v[164:165]\n"
  "v_pk_add_f32 v[110:111], v[110:111], v[166:167]\n"
  "s_waitcnt lgkmcnt(0)\n"
  "v_pk_add_f32 v[104:105], v[104:105], v[168:169]\n"
  "v_pk_add_f32 v[106:107], v[106:107], v[170:171]\n"
  "v_pk_add_f32 v[108:109], v[108:109], v[172:173]\n"
  "v_pk_add_f32 v[110:111], v[110:111], v[174:175]\n"
  "v_cvt_pk_bf16_f32 v52, v104, v105\n"
  "v_cvt_pk_bf16_f32 v53, v106, v107\n"
  "v_cvt_pk_bf16_f32 v54, v108, v109\n"
  "v_cvt_pk_bf16_f32 v55, v110, v111\n"
  "s_nop 0\n"
  "v_permlane32_swap_b32 v52, v54\n"
  "v_permlane32_swap_b32 v53, v55\n"
  "buffer_store_dwordx4 v[52:55], v15, s[40:43], s58 offen\n"
  "s_endpgm\n"
  ".La0_f:\n"
  "s_call_b64 s[46:47], .La0\n"
  "v_xor_b32 v5, s100, v131\n"
  "v_bitop3_b32 v5, v135, s100, v5 bitop3:0xbe\n"
  "v_bitop3_b32 v5, v139, s100, v5 bitop3:0xbe\n"
  "v_bitop3_b32 v5, v143, s100, v5 bitop3:0xbe\n"
  "v_cmp_lt_u32 vcc, 0xffff, v5\n"
  "s_and_b64 vcc, vcc, s[48:49]\n"
  "s_cbranch_scc1 .La0_f\n"
  "s_branch .La0_k\n"
  ".La1_f:\n"
  "s_call_b64 s[46:47], .La1\n"
  "v_xor_b32 v5, s100, v147\n"
  "v_bitop3_b32 v5, v151, s100, v5 bitop3:0xbe\n"
  "v_bitop3_b32 v5, v155, s100, v5 bitop3:0xbe\n"
  "v_bitop3_b32 v5, v159, s100, v5 bitop3:0xbe\n"
  "v_cmp_lt_u32 vcc, 0xffff, v5\n"
  "s_and_b64 vcc, vcc, s[48:49]\n"
  "s_cbranch_scc1 .La1_f\n"
  "s_branch .La1_k\n"
  ".La2_f:\n"
  "s_call_b64 s[46:47], .La2\n"
  "v_xor_b32 v5, s100, v163\n"
  "v_bitop3_b32 v5, v167, s100, v5 bitop3:0xbe\n"
  "v_bitop3_b32 v5, v171, s100, v5 bitop3:0xbe\n"
  "v_bitop3_b32 v5, v175, s100, v5 bitop3:0xbe\n"
  "v_cmp_lt_u32 vcc, 0xffff, v5\n"
  "s_and_b64 vcc, vcc, s[48:49]\n"
  "s_cbranch_scc1 .La2_f\n"
  "s_branch .La2_k\n"
  ".La3_f:\n"
  "s_call_b64 s[46:47], .La3\n"
  "v_xor_b32 v5, s100, v179\n"
  "v_bitop3_b32 v5, v183, s100, v5 bitop3:0xbe\n"
  "v_bitop3_b32 v5, v187, s100, v5 bitop3:0xbe\n"
  "v_bitop3_b32 v5, v191, s100, v5 bitop3:0xbe\n"
  "v_cmp_lt_u32 vcc, 0xffff, v5\n"
  "s_and_b64 vcc, vcc, s[48:49]\n"
  "s_cbranch_scc1 .La3_f\n"
  "s_branch .La3_k\n"
  ".Lb0_f:\n"
  "s_call_b64 s[46:47], .Lb0\n"
  "v_xor_b32 v5, s100, v131\n"
  "v_bitop3_b32 v5, v135, s100, v5 bitop3:0xbe\n"
  "v_bitop3_b32 v5, v139, s100, v5 bitop3:0xbe\n"
  "v_bitop3_b32 v5, v143, s100, v5 bitop3:0xbe\n"
  "v_cmp_lt_u32 vcc, 0xffff, v5\n"
  "s_and_b64 vcc, vcc, s[48:49]\n"
  "s_cbranch_scc1 .Lb0_f\n"
  "s_branch .Lb0_k\n"
  ".Lb1_f:\n"
  "s_call_b64 s[46:47], .Lb1\n"
  "v_xor_b32 v5, s100, v147\n"
  "v_bitop3_b32 v5, v151, s100, v5 bitop3:0xbe\n"
  "v_bitop3_b32 v5, v155, s100, v5 bitop3:0xbe\n"
  "v_bitop3_b32 v5, v159, s100, v5 bitop3:0xbe\n"
  "v_cmp_lt_u32 vcc, 0xffff, v5\n"
  "s_and_b64 vcc, vcc, s[48:49]\n"
  "s_cbranch_scc1 .Lb1_f\n"
  "s_branch .Lb1_k\n"
  ".Lb2_f:\n"
  "s_call_b64 s[46:47], .Lb2\n"
  "v_xor_b32 v5, s100, v163\n"
  "v_bitop3_b32 v5, v167, s100, v5 bitop3:0xbe\n"
  "v_bitop3_b32 v5, v171, s100, v5 bitop3:0xbe\n"
  "v_bitop3_b32 v5, v175, s100, v5 bitop3:0xbe\n"
  "v_cmp_lt_u32 vcc, 0xffff, v5\n"
  "s_and_b64 vcc, vcc, s[48:49]\n"
  "s_cbranch_scc1 .Lb2_f\n"
  "s_branch .Lb2_k\n"
  ".Lb3_f:\n"
  "s_call_b64 s[46:47], .Lb3\n"
  "v_xor_b32 v5, s100, v179\n"
  "v_bitop3_b32 v5, v183, s100, v5 bitop3:0xbe\n"
  "v_bitop3_b32 v5, v187, s100, v5 bitop3:0xbe\n"
  "v_bitop3_b32 v5, v191, s100, v5 bitop3:0xbe\n"
  "v_cmp_lt_u32 vcc, 0xffff, v5\n"
  "s_and_b64 vcc, vcc, s[48:49]\n"
  "s_cbranch_scc1 .Lb3_f\n"
  "s_branch .Lb3_k\n"
  ".Lc0_f:\n"
  "s_call_b64 s[46:47], .Lc0\n"
  "v_max3_u32 v5, v128, v130, v132\n"
  "v_max3_u32 v5, v5, v134, v136\n"
  "v_max3_u32 v5, v5, v138, v140\n"
  "v_max3_u32 v5, v5, v142, v142\n"
  "v_cmp_eq_u32 vcc, -1, v5\n"
  "s_and_b64 vcc, vcc, s[48:49]\n"
  "s_cbranch_scc1 .Lc0_f\n"
  "s_branch .Lc0_k\n"
  ".Lc1_f:\n"
  "s_call_b64 s[46:47], .Lc1\n"
  "v_max3_u32 v5, v144, v146, v148\n"
  "v_max3_u32 v5, v5, v150, v152\n"
  "v_max3_u32 v5, v5, v154, v156\n"
  "v_max3_u32 v5, v5, v158, v158\n"
  "v_cmp_eq_u32 vcc, -1, v5\n"
  "s_and_b64 vcc, vcc, s[48:49]\n"
  "s_cbranch_scc1 .Lc1_f\n"
  "s_branch .Lc1_k\n"
  ".Lc2_f:\n"
  "s_call_b64 s[46:47], .Lc2\n"
  "v_max3_u32 v5, v160, v162, v164\n"
  "v_max3_u32 v5, v5, v166, v168\n"
  "v_max3_u32 v5, v5, v170, v172\n"
  "v_max3_u32 v5, v5, v174, v174\n"
  "v_cmp_eq_u32 vcc, -1, v5\n"
  "s_and_b64 vcc, vcc, s[48:49]\n"
  "s_cbranch_scc1 .Lc2_f\n"
  "s_branch .Lc2_k\n"
  ".Lc3_f:\n"
  "s_call_b64 s[46:47], .Lc3\n"
  "v_max3_u32 v5, v176, v178, v180\n"
  "v_max3_u32 v5, v5, v182, v184\n"
  "v_max3_u32 v5, v5, v186, v188\n"
  "v_max3_u32 v5, v5, v190, v190\n"
  "v_cmp_eq_u32 vcc, -1, v5\n"
  "s_and_b64 vcc, vcc, s[48:49]\n"
  "s_cbranch_scc1 .Lc3_f\n"
  "s_branch .Lc3_k\n"
  ".Lc4_f:\n"
  "s_call_b64 s[46:47], .Lc4\n"
  "v_max3_u32 v5, v192, v194, v196\n"
  "v_max3_u32 v5, v5, v198, v200\n"
  "v_max3_u32 v5, v5, v202, v204\n"
  "v_max3_u32 v5, v5, v206, v206\n"
  "v_cmp_eq_u32 vcc, -1, v5\n"
  "s_and_b64 vcc, vcc, s[48:49]\n"
  "s_cbranch_scc1 .Lc4_f\n"
  "s_branch .Lc4_k\n"
  ".Lc5_f:\n"
  "s_call_b64 s[46:47], .Lc5\n"
  "v_max3_u32 v5, v208, v210, v212\n"
  "v_max3_u32 v5, v5, v214, v216\n"
  "v_max3_u32 v5, v5, v218, v220\n"
  "v_max3_u32 v5, v5, v222, v222\n"
  "v_cmp_eq_u32 vcc, -1, v5\n"
  "s_and_b64 vcc, vcc, s[48:49]\n"
  "s_cbranch_scc1 .Lc5_f\n"
  "s_branch .Lc5_k\n"
  ".Lc6_f:\n"
  "s_call_b64 s[46:47], .Lc6\n"
  "v_max3_u32 v5, v224, v226, v228\n"
  "v_max3_u32 v5, v5, v230, v232\n"
  "v_max3_u32 v5, v5, v234, v236\n"
  "v_max3_u32 v5, v5, v238, v238\n"
  "v_cmp_eq_u32 vcc, -1, v5\n"
  "s_and_b64 vcc, vcc, s[48:49]\n"
  "s_cbranch_scc1 .Lc6_f\n"
  "s_branch .Lc6_k\n"
  ".Lc7_f:\n"
  "s_call_b64 s[46:47], .Lc7\n"
  "v_max3_u32 v5, v48, v50, v52\n"
  "v_max3_u32 v5, v5, v54, v56\n"
  "v_max3_u32 v5, v5, v58, v60\n"
  "v_max3_u32 v5, v5, v62, v240\n"
  "v_max3_u32 v5, v5, v242, v244\n"
  "v_max3_u32 v5, v5, v246, v248\n"
  "v_max3_u32 v5, v5, v250, v250\n"
  "v_cmp_eq_u32 vcc, -1, v5\n"
  "s_and_b64 vcc, vcc, s[48:49]\n"
  "s_cbranch_scc1 .Lc7_f\n"
  "s_branch .Lc7_k\n"
  ".Ld0_0_f:\n"
  "s_call_b64 s[46:47], .Ld0_0\n"
  "v_xor_b32 v5, s50, v240\n"
  "v_bitop3_b32 v5, v241, s51, v5 bitop3:0xbe\n"
  "v_bitop3_b32 v5, v242, s93, v5 bitop3:0xbe\n"
  "v_bitop3_b32 v5, v243, s101, v5 bitop3:0xbe\n"
  "v_bitop3_b32 v5, v244, s50, v5 bitop3:0xbe\n"
  "v_bitop3_b32 v5, v245, s51, v5 bitop3:0xbe\n"
  "v_bitop3_b32 v5, v246, s93, v5 bitop3:0xbe\n"
  "v_bitop3_b32 v5, v247, s101, v5 bitop3:0xbe\n"
  "v_bitop3_b32 v5, v248, s50, v5 bitop3:0xbe\n"
  "v_bitop3_b32 v5, v249, s51, v5 bitop3:0xbe\n"
  "v_bitop3_b32 v5, v250, s93, v5 bitop3:0xbe\n"
  "v_bitop3_b32 v5, v251, s101, v5 bitop3:0xbe\n"
  "v_bitop3_b32 v5, v112, s50, v5 bitop3:0xbe\n"
  "v_bitop3_b32 v5, v113, s51, v5 bitop3:0xbe\n"
  "v_bitop3_b32 v5, v114, s93, v5 bitop3:0xbe\n"
  "v_bitop3_b32 v5, v115, s101, v5 bitop3:0xbe\n"
  "v_and_b32 v5, 0x80008000, v5\n"
  "v_cmp_ne_u32 vcc, 0, v5\n"
  "s_and_b64 vcc, vcc, s[48:49]\n"
  "s_cbranch_scc1 .Ld0_0_f\n"
  "s_branch .Ld0_0_k\n"
  ".Ld0_1_f:\n"
  "s_call_b64 s[46:47], .Ld0_1\n"
  "v_xor_b32 v5, s50, v116\n"
  "v_bitop3_b32 v5, v117, s51, v5 bitop3:0xbe\n"
  "v_bitop3_b32 v5, v118, s93, v5 bitop3:0xbe\n"
  "v_bitop3_b32 v5, v119, s101, v5 bitop3:0xbe\n"
  "v_bitop3_b32 v5, v120, s50, v5 bitop3:0xbe\n"
  "v_bitop3_b32 v5, v121, s51, v5 bitop3:0xbe\n"
  "v_bitop3_b32 v5, v122, s93, v5 bitop3:0xbe\n"
  "v_bitop3_b32 v5, v123, s101, v5 bitop3:0xbe\n"
  "v_bitop3_b32 v5, v128, s50, v5 bitop3:0xbe\n"
  "v_bitop3_b32 v5, v129, s51, v5 bitop3:0xbe\n"
  "v_bitop3_b32 v5, v130, s93, v5 bitop3:0xbe\n"
  "v_bitop3_b32 v5, v131, s101, v5 bitop3:0xbe\n"
  "v_bitop3_b32 v5, v132, s50, v5 bitop3:0xbe\n"
  "v_bitop3_b32 v5, v133, s51, v5 bitop3:0xbe\n"
  "v_bitop3_b32 v5, v134, s93, v5 bitop3:0xbe\n"
  "v_bitop3_b32 v5, v135, s101, v5 bitop3:0xbe\n"
  "v_and_b32 v5, 0x80008000, v5\n"
  "v_cmp_ne_u32 vcc, 0, v5\n"
  "s_and_b64 vcc, vcc, s[48:49]\n"
  "s_cbranch_scc1 .Ld0_1_f\n"
  "s_branch .Ld0_1_k\n"
  ".Ld0_2_f:\n"
  "s_call_b64 s[46:47], .Ld0_2\n"
  "v_xor_b32 v5, s50, v136\n"
  "v_bitop3_b32 v5, v137, s51, v5 bitop3:0xbe\n"
  "v_bitop3_b32 v5, v138, s93, v5 bitop3:0xbe\n"
  "v_bitop3_b32 v5, v139, s101, v5 bitop3:0xbe\n"
  "v_bitop3_b32 v5, v140, s50, v5 bitop3:0xbe\n"
  "v_bitop3_b32 v5, v141, s51, v5 bitop3:0xbe\n"
  "v_bitop3_b32 v5, v142, s93, v5 bitop3:0xbe\n"
  "v_bitop3_b32 v5, v143, s101, v5 bitop3:0xbe\n"
  "v_bitop3_b32 v5, v144, s50, v5 bitop3:0xbe\n"
  "v_bitop3_b32 v5, v145, s51, v5 bitop3:0xbe\n"
  "v_bitop3_b32 v5, v146, s93, v5 bitop3:0xbe\n"
  "v_bitop3_b32 v5, v147, s101, v5 bitop3:0xbe\n"
  "v_bitop3_b32 v5, v148, s50, v5 bitop3:0xbe\n"
  "v_bitop3_b32 v5, v149, s51, v5 bitop3:0xbe\n"
  "v_bitop3_b32 v5, v150, s93, v5 bitop3:0xbe\n"
  "v_bitop3_b32 v5, v151, s101, v5 bitop3:0xbe\n"
  "v_and_b32 v5, 0x80008000, v5\n"
  "v_cmp_ne_u32 vcc, 0, v5\n"
  "s_and_b64 vcc, vcc, s[48:49]\n"
  "s_cbranch_scc1 .Ld0_2_f\n"
  "s_branch .Ld0_2_k\n"
  ".Ld0_3_f:\n"
  "s_call_b64 s[46:47], .Ld0_3\n"
  "v_xor_b32 v5, s50, v152\n"
  "v_bitop3_b32 v5, v153, s51, v5 bitop3:0xbe\n"
  "v_bitop3_b32 v5, v154, s93, v5 bitop3:0xbe\n"
  "v_bitop3_b32 v5, v155, s101, v5 bitop3:0xbe\n"
  "v_bitop3_b32 v5, v156, s50, v5 bitop3:0xbe\n"
  "v_bitop3_b32 v5, v157, s51, v5 bitop3:0xbe\n"
  "v_bitop3_b32 v5, v158, s93, v5 bitop3:0xbe\n"
  "v_bitop3_b32 v5, v159, s101, v5 bitop3:0xbe\n"
  "v_bitop3_b32 v5, v160, s50, v5 bitop3:0xbe\n"
  "v_bitop3_b32 v5, v161, s51, v5 bitop3:0xbe\n"
  "v_bitop3_b32 v5, v162, s93, v5 bitop3:0xbe\n"
  "v_bitop3_b32 v5, v163, s101, v5 bitop3:0xbe\n"
  "v_bitop3_b32 v5, v164, s50, v5 bitop3:0xbe\n"
  "v_bitop3_b32 v5, v165, s51, v5 bitop3:0xbe\n"
  "v_bitop3_b32 v5, v166, s93, v5 bitop3:0xbe\n"
  "v_bitop3_b32 v5, v167, s101, v5 bitop3:0xbe\n"
  "v_and_b32 v5, 0x80008000, v5\n"
  "v_cmp_ne_u32 vcc, 0, v5\n"
  "s_and_b64 vcc, vcc, s[48:49]\n"
  "s_cbranch_scc1 .Ld0_3_f\n"
  "s_branch .Ld0_3_k\n"
  ".Ld1_0_f:\n"
  "s_call_b64 s[46:47], .Ld1_0\n"
  "v_xor_b32 v5, s50, v168\n"
  "v_bitop3_b32 v5, v169, s51, v5 bitop3:0xbe\n"
  "v_bitop3_b32 v5, v170, s93, v5 bitop3:0xbe\n"
  "v_bitop3_b32 v5, v171, s101, v5 bitop3:0xbe\n"
  "v_bitop3_b32 v5, v172, s50, v5 bitop3:0xbe\n"
  "v_bitop3_b32 v5, v173, s51, v5 bitop3:0xbe\n"
  "v_bitop3_b32 v5, v174, s93, v5 bitop3:0xbe\n"
  "v_bitop3_b32 v5, v175, s101, v5 bitop3:0xbe\n"
  "v_bitop3_b32 v5, v176, s50, v5 bitop3:0xbe\n"
  "v_bitop3_b32 v5, v177, s51, v5 bitop3:0xbe\n"
  "v_bitop3_b32 v5, v178, s93, v5 bitop3:0xbe\n"
  "v_bitop3_b32 v5, v179, s101, v5 bitop3:0xbe\n"
  "v_bitop3_b32 v5, v180, s50, v5 bitop3:0xbe\n"
  "v_bitop3_b32 v5, v181, s51, v5 bitop3:0xbe\n"
  "v_bitop3_b32 v5, v182, s93, v5 bitop3:0xbe\n"
  "v_bitop3_b32 v5, v183, s101, v5 bitop3:0xbe\n"
  "v_and_b32 v5, 0x80008000, v5\n"
  "v_cmp_ne_u32 vcc, 0, v5\n"
  "s_and_b64 vcc, vcc, s[48:49]\n"
  "s_cbranch_scc1 .Ld1_0_f\n"
  "s_branch .Ld1_0_k\n"
  ".Ld1_1_f:\n"
  "s_call_b64 s[46:47], .Ld1_1\n"
  "v_xor_b32 v5, s50, v184\n"
  "v_bitop3_b32 v5, v185, s51, v5 bitop3:0xbe\n"
  "v_bitop3_b32 v5, v186, s93, v5 bitop3:0xbe\n"
  "v_bitop3_b32 v5, v187, s101, v5 bitop3:0xbe\n"
  "v_bitop3_b32 v5, v188, s50, v5 bitop3:0xbe\n"
  "v_bitop3_b32 v5, v189, s51, v5 bitop3:0xbe\n"
  "v_bitop3_b32 v5, v190, s93, v5 bitop3:0xbe\n"
  "v_bitop3_b32 v5, v191, s101, v5 bitop3:0xbe\n"
  "v_bitop3_b32 v5, v192, s50, v5 bitop3:0xbe\n"
  "v_bitop3_b32 v5, v193, s51, v5 bitop3:0xbe\n"
  "v_bitop3_b32 v5, v194, s93, v5 bitop3:0xbe\n"
  "v_bitop3_b32 v5, v195, s101, v5 bitop3:0xbe\n"
  "v_bitop3_b32 v5, v196, s50, v5 bitop3:0xbe\n"
  "v_bitop3_b32 v5, v197, s51, v5 bitop3:0xbe\n"
  "v_bitop3_b32 v5, v198, s93, v5 bitop3:0xbe\n"
  "v_bitop3_b32 v5, v199, s101, v5 bitop3:0xbe\n"
  "v_and_b32 v5, 0x80008000, v5\n"
  "v_cmp_ne_u32 vcc, 0, v5\n"
  "s_and_b64 vcc, vcc, s[48:49]\n"
  "s_cbranch_scc1 .Ld1_1_f\n"
  "s_branch .Ld1_1_k\n"
  ".Ld1_2_f:\n"
  "s_call_b64 s[46:47], .Ld1_2\n"
  "v_xor_b32 v5, s50, v200\n"
  "v_bitop3_b32 v5, v201, s51, v5 bitop3:0xbe\n"
  "v_bitop3_b32 v5, v202, s93, v5 bitop3:0xbe\n"
  "v_bitop3_b32 v5, v203, s101, v5 bitop3:0xbe\n"
  "v_bitop3_b32 v5, v204, s50, v5 bitop3:0xbe\n"
  "v_bitop3_b32 v5, v205, s51, v5 bitop3:0xbe\n"
  "v_bitop3_b32 v5, v206, s93, v5 bitop3:0xbe\n"
  "v_bitop3_b32 v5, v207, s101, v5 bitop3:0xbe\n"
  "v_bitop3_b32 v5, v208, s50, v5 bitop3:0xbe\n"
  "v_bitop3_b32 v5, v209, s51, v5 bitop3:0xbe\n"
  "v_bitop3_b32 v5, v210, s93, v5 bitop3:0xbe\n"
  "v_bitop3_b32 v5, v211, s101, v5 bitop3:0xbe\n"
  "v_bitop3_b32 v5, v212, s50, v5 bitop3:0xbe\n"
  "v_bitop3_b32 v5, v213, s51, v5 bitop3:0xbe\n"
  "v_bitop3_b32 v5, v214, s93, v5 bitop3:0xbe\n"
  "v_bitop3_b32 v5, v215, s101, v5 bitop3:0xbe\n"
  "v_and_b32 v5, 0x80008000, v5\n"
  "v_cmp_ne_u32 vcc, 0, v5\n"
  "s_and_b64 vcc, vcc, s[48:49]\n"
  "s_cbranch_scc1 .Ld1_2_f\n"
  "s_branch .Ld1_2_k\n"
  ".Ld1_3_f:\n"
  "s_call_b64 s[46:47], .Ld1_3\n"
  "v_xor_b32 v5, s50, v216\n"
  "v_bitop3_b32 v5, v217, s51, v5 bitop3:0xbe\n"
  "v_bitop3_b32 v5, v218, s93, v5 bitop3:0xbe\n"
  "v_bitop3_b32 v5, v219, s101, v5 bitop3:0xbe\n"
  "v_bitop3_b32 v5, v220, s50, v5 bitop3:0xbe\n"
  "v_bitop3_b32 v5, v221, s51, v5 bitop3:0xbe\n"
  "v_bitop3_b32 v5, v222, s93, v5 bitop3:0xbe\n"
  "v_bitop3_b32 v5, v223, s101, v5 bitop3:0xbe\n"
  "v_bitop3_b32 v5, v224, s50, v5 bitop3:0xbe\n"
  "v_bitop3_b32 v5, v225, s51, v5 bitop3:0xbe\n"
  "v_bitop3_b32 v5, v226, s93, v5 bitop3:0xbe\n"
  "v_bitop3_b32 v5, v227, s101, v5 bitop3:0xbe\n"
  "v_bitop3_b32 v5, v228, s50, v5 bitop3:0xbe\n"
  "v_bitop3_b32 v5, v229, s51, v5 bitop3:0xbe\n"
  "v_bitop3_b32 v5, v230, s93, v5 bitop3:0xbe\n"
  "v_bitop3_b32 v5, v231, s101, v5 bitop3:0xbe\n"
  "v_and_b32 v5, 0x80008000, v5\n"
  "v_cmp_ne_u32 vcc, 0, v5\n"
  "s_and_b64 vcc, vcc, s[48:49]\n"
  "s_cbranch_scc1 .Ld1_3_f\n"
  "s_branch .Ld1_3_k\n"
  ".Ld2_0_f:\n"
  "s_call_b64 s[46:47], .Ld2_0\n"
  "v_xor_b32 v5, s50, v232\n"
  "v_bitop3_b32 v5, v233, s51, v5 bitop3:0xbe\n"
  "v_bitop3_b32 v5, v234, s93, v5 bitop3:0xbe\n"
  "v_bitop3_b32 v5, v235, s101, v5 bitop3:0xbe\n"
  "v_bitop3_b32 v5, v236, s50, v5 bitop3:0xbe\n"
  "v_bitop3_b32 v5, v237, s51, v5 bitop3:0xbe\n"
  "v_bitop3_b32 v5, v238, s93, v5 bitop3:0xbe\n"
  "v_bitop3_b32 v5, v239, s101, v5 bitop3:0xbe\n"
  "v_bitop3_b32 v5, v64, s50, v5 bitop3:0xbe\n"
  "v_bitop3_b32 v5, v65, s51, v5 bitop3:0xbe\n"
  "v_bitop3_b32 v5, v66, s93, v5 bitop3:0xbe\n"
  "v_bitop3_b32 v5, v67, s101, v5 bitop3:0xbe\n"
  "v_bitop3_b32 v5, v68, s50, v5 bitop3:0xbe\n"
  "v_bitop3_b32 v5, v69, s51, v5 bitop3:0xbe\n"
  "v_bitop3_b32 v5, v70, s93, v5 bitop3:0xbe\n"
  "v_bitop3_b32 v5, v71, s101, v5 bitop3:0xbe\n"
  "v_and_b32 v5, 0x80008000, v5\n"
  "v_cmp_ne_u32 vcc, 0, v5\n"
  "s_and_b64 vcc, vcc, s[48:49]\n"
  "s_cbranch_scc1 .Ld2_0_f\n"
  "s_branch .Ld2_0_k\n"
  ".Ld2_1_f:\n"
  "s_call_b64 s[46:47], .Ld2_1\n"
  "v_xor_b32 v5, s50, v72\n"
  "v_bitop3_b32 v5, v73, s51, v5 bitop3:0xbe\n"
  "v_bitop3_b32 v5, v74, s93, v5 bitop3:0xbe\n"
  "v_bitop3_b32 v5, v75, s101, v5 bitop3:0xbe\n"
  "v_bitop3_b32 v5, v76, s50, v5 bitop3:0xbe\n"
  "v_bitop3_b32 v5, v77, s51, v5 bitop3:0xbe\n"
  "v_bitop3_b32 v5, v78, s93, v5 bitop3:0xbe\n"
  "v_bitop3_b32 v5, v79, s101, v5 bitop3:0xbe\n"
  "v_bitop3_b32 v5, v80, s50, v5 bitop3:0xbe\n"
  "v_bitop3_b32 v5, v81, s51, v5 bitop3:0xbe\n"
  "v_bitop3_b32 v5, v82, s93, v5 bitop3:0xbe\n"
  "v_bitop3_b32 v5, v83, s101, v5 bitop3:0xbe\n"
  "v_bitop3_b32 v5, v84, s50, v5 bitop3:0xbe\n"
  "v_bitop3_b32 v5, v85, s51, v5 bitop3:0xbe\n"
  "v_bitop3_b32 v5, v86, s93, v5 bitop3:0xbe\n"
  "v_bitop3_b32 v5, v87, s101, v5 bitop3:0xbe\n"
  "v_and_b32 v5, 0x80008000, v5\n"
  "v_cmp_ne_u32 vcc, 0, v5\n"
  "s_and_b64 vcc, vcc, s[48:49]\n"
  "s_cbranch_scc1 .Ld2_1_f\n"
  "s_branch .Ld2_1_k\n"
  ".Ld2_2_f:\n"
  "s_call_b64 s[46:47], .Ld2_2\n"
  "v_xor_b32 v5, s50, v88\n"
  "v_bitop3_b32 v5, v89, s51, v5 bitop3:0xbe\n"
  "v_bitop3_b32 v5, v90, s93, v5 bitop3:0xbe\n"
  "v_bitop3_b32 v5, v91, s101, v5 bitop3:0xbe\n"
  "v_bitop3_b32 v5, v92, s50, v5 bitop3:0xbe\n"
  "v_bitop3_b32 v5, v93, s51, v5 bitop3:0xbe\n"
  "v_bitop3_b32 v5, v94, s93, v5 bitop3:0xbe\n"
  "v_bitop3_b32 v5, v95, s101, v5 bitop3:0xbe\n"
  "v_bitop3_b32 v5, v48, s50, v5 bitop3:0xbe\n"
  "v_bitop3_b32 v5, v49, s51, v5 bitop3:0xbe\n"
  "v_bitop3_b32 v5, v50, s93, v5 bitop3:0xbe\n"
  "v_bitop3_b32 v5, v51, s101, v5 bitop3:0xbe\n"
  "v_bitop3_b32 v5, v52, s50, v5 bitop3:0xbe\n"
  "v_bitop3_b32 v5, v53, s51, v5 bitop3:0xbe\n"
  "v_bitop3_b32 v5, v54, s93, v5 bitop3:0xbe\n"
  "v_bitop3_b32 v5, v55, s101, v5 bitop3:0xbe\n"
  "v_and_b32 v5, 0x80008000, v5\n"
  "v_cmp_ne_u32 vcc, 0, v5\n"
  "s_and_b64 vcc, vcc, s[48:49]\n"
  "s_cbranch_scc1 .Ld2_2_f\n"
  "s_branch .Ld2_2_k\n"
  ".Ld2_3_f:\n"
  "s_call_b64 s[46:47], .Ld2_3\n"
  "v_xor_b32 v5, s50, v56\n"
  "v_bitop3_b32 v5, v57, s51, v5 bitop3:0xbe\n"
  "v_bitop3_b32 v5, v58, s93, v5 bitop3:0xbe\n"
  "v_bitop3_b32 v5, v59, s101, v5 bitop3:0xbe\n"
  "v_bitop3_b32 v5, v60, s50, v5 bitop3:0xbe\n"
  "v_bitop3_b32 v5, v61, s51, v5 bitop3:0xbe\n"
  "v_bitop3_b32 v5, v62, s93, v5 bitop3:0xbe\n"
  "v_bitop3_b32 v5, v63, s101, v5 bitop3:0xbe\n"
  "v_bitop3_b32 v5, v240, s50, v5 bitop3:0xbe\n"
  "v_bitop3_b32 v5, v241, s51, v5 bitop3:0xbe\n"
  "v_bitop3_b32 v5, v242, s93, v5 bitop3:0xbe\n"
  "v_bitop3_b32 v5, v243, s101, v5 bitop3:0xbe\n"
  "v_bitop3_b32 v5, v244, s50, v5 bitop3:0xbe\n"
  "v_bitop3_b32 v5, v245, s51, v5 bitop3:0xbe\n"
  "v_bitop3_b32 v5, v246, s93, v5 bitop3:0xbe\n"
  "v_bitop3_b32 v5, v247, s101, v5 bitop3:0xbe\n"
  "v_and_b32 v5, 0x80008000, v5\n"
  "v_cmp_ne_u32 vcc, 0, v5\n"
  "s_and_b64 vcc, vcc, s[48:49]\n"
  "s_cbranch_scc1 .Ld2_3_f\n"
  "s_branch .Ld2_3_k\n"
  ".Ld3_0_f:\n"
  "s_call_b64 s[46:47], .Ld3_0\n"
  "v_xor_b32 v5, s50, v248\n"
  "v_bitop3_b32 v5, v249, s51, v5 bitop3:0xbe\n"
  "v_bitop3_b32 v5, v250, s93, v5 bitop3:0xbe\n"
  "v_bitop3_b32 v5, v251, s101, v5 bitop3:0xbe\n"
  "v_bitop3_b32 v5, v112, s50, v5 bitop3:0xbe\n"
  "v_bitop3_b32 v5, v113, s51, v5 bitop3:0xbe\n"
  "v_bitop3_b32 v5, v114, s93, v5 bitop3:0xbe\n"
  "v_bitop3_b32 v5, v115, s101, v5 bitop3:0xbe\n"
  "v_bitop3_b32 v5, v116, s50, v5 bitop3:0xbe\n"
  "v_bitop3_b32 v5, v117, s51, v5 bitop3:0xbe\n"
  "v_bitop3_b32 v5, v118, s93, v5 bitop3:0xbe\n"
  "v_bitop3_b32 v5, v119, s101, v5 bitop3:0xbe\n"
  "v_bitop3_b32 v5, v120, s50, v5 bitop3:0xbe\n"
  "v_bitop3_b32 v5, v121, s51, v5 bitop3:0xbe\n"
  "v_bitop3_b32 v5, v122, s93, v5 bitop3:0xbe\n"
  "v_bitop3_b32 v5, v123, s101, v5 bitop3:0xbe\n"
  "v_and_b32 v5, 0x80008000, v5\n"
  "v_cmp_ne_u32 vcc, 0, v5\n"
  "s_and_b64 vcc, vcc, s[48:49]\n"
  "s_cbranch_scc1 .Ld3_0_f\n"
  "s_branch .Ld3_0_k\n"
  ".Ld3_1_f:\n"
  "s_call_b64 s[46:47], .Ld3_1\n"
  "v_xor_b32 v5, s50, v128\n"
  "v_bitop3_b32 v5, v129, s51, v5 bitop3:0xbe\n"
  "v_bitop3_b32 v5, v130, s93, v5 bitop3:0xbe\n"
  "v_bitop3_b32 v5, v131, s101, v5 bitop3:0xbe\n"
  "v_bitop3_b32 v5, v132, s50, v5 bitop3:0xbe\n"
  "v_bitop3_b32 v5, v133, s51, v5 bitop3:0xbe\n"
  "v_bitop3_b32 v5, v134, s93, v5 bitop3:0xbe\n"
  "v_bitop3_b32 v5, v135, s101, v5 bitop3:0xbe\n"
  "v_bitop3_b32 v5, v136, s50, v5 bitop3:0xbe\n"
  "v_bitop3_b32 v5, v137, s51, v5 bitop3:0xbe\n"
  "v_bitop3_b32 v5, v138, s93, v5 bitop3:0xbe\n"
  "v_bitop3_b32 v5, v139, s101, v5 bitop3:0xbe\n"
  "v_bitop3_b32 v5, v140, s50, v5 bitop3:0xbe\n"
  "v_bitop3_b32 v5, v141, s51, v5 bitop3:0xbe\n"
  "v_bitop3_b32 v5, v142, s93, v5 bitop3:0xbe\n"
  "v_bitop3_b32 v5, v143, s101, v5 bitop3:0xbe\n"
  "v_and_b32 v5, 0x80008000, v5\n"
  "v_cmp_ne_u32 vcc, 0, v5\n"
  "s_and_b64 vcc, vcc, s[48:49]\n"
  "s_cbranch_scc1 .Ld3_1_f\n"
  "s_branch .Ld3_1_k\n"
  ".La0:\n"
  "s_add_u32 s77, s53, 0\n"
  "s_and_b32 s77, s77, 3\n"
  "s_lshl_b32 s77, s77, 12\n"
  "s_add_u32 s77, s77, s92\n"
  "buffer_load_dwordx4 v[128:131], v2, s[12:15], s77 offen offset:0 sc1\n"
  "buffer_load_dwordx4 v[132:135], v2, s[12:15], s77 offen offset:1024 sc1\n"
  "buffer_load_dwordx4 v[136:139], v2, s[12:15], s77 offen offset:2048 sc1\n"
  "buffer_load_dwordx4 v[140:143], v2, s[12:15], s77 offen offset:3072 sc1\n"
  ".La1:\n"
  "s_add_u32 s77, s53, 1\n"
  "s_and_b32 s77, s77, 3\n"
  "s_lshl_b32 s77, s77, 12\n"
  "s_add_u32 s77, s77, s92\n"
  "buffer_load_dwordx4 v[144:147], v2, s[12:15], s77 offen offset:0 sc1\n"
  "buffer_load_dwordx4 v[148:151], v2, s[12:15], s77 offen offset:1024 sc1\n"
  "buffer_load_dwordx4 v[152:155], v2, s[12:15], s77 offen offset:2048 sc1\n"
  "buffer_load_dwordx4 v[156:159], v2, s[12:15], s77 offen offset:3072 sc1\n"
  ".La2:\n"
  "s_add_u32 s77, s53, 2\n"
  "s_and_b32 s77, s77, 3\n"
  "s_lshl_b32 s77, s77, 12\n"
  "s_add_u32 s77, s77, s92\n"
  "buffer_load_dwordx4 v[160:163], v2, s[12:15], s77 offen offset:0 sc1\n"
  "buffer_load_dwordx4 v[164:167], v2, s[12:15], s77 offen offset:1024 sc1\n"
  "buffer_load_dwordx4 v[168:171], v2, s[12:15], s77 offen offset:2048 sc1\n"
  "buffer_load_dwordx4 v[172:175], v2, s[12:15], s77 offen offset:3072 sc1\n"
  ".La3:\n"
  "s_add_u32 s77, s53, 3\n"
  "s_and_b32 s77, s77, 3\n"
  "s_lshl_b32 s77, s77, 12\n"
  "s_add_u32 s77, s77, s92\n"
  "buffer_load_dwordx4 v[176:179], v2, s[12:15], s77 offen offset:0 sc1\n"
  "buffer_load_dwordx4 v[180:183], v2, s[12:15], s77 offen offset:1024 sc1\n"
  "buffer_load_dwordx4 v[184:187], v2, s[12:15], s77 offen offset:2048 sc1\n"
  "buffer_load_dwordx4 v[188:191], v2, s[12:15], s77 offen offset:3072 sc1\n"
  "s_branch .Lret\n"
  ".Lb0:\n"
  "s_add_u32 s77, s57, 0\n"
  "s_and_b32 s77, s77, 3\n"
  "s_lshl_b32 s77, s77, 11\n"
  "s_add_u32 s77, s77, s92\n"
  "buffer_load_dwordx4 v[128:131], v2, s[12:15], s77 offen offset:0 sc1\n"
  "buffer_load_dwordx4 v[132:135], v2, s[12:15], s77 offen offset:1024 sc1\n"
  "buffer_load_dwordx4 v[136:139], v206, s[12:15], s77 offen offset:0 sc1\n"
  "buffer_load_dwordx4 v[140:143], v206, s[12:15], s77 offen offset:1024 sc1\n"
  ".Lb1:\n"
  "s_add_u32 s77, s57, 1\n"
  "s_and_b32 s77, s77, 3\n"
  "s_lshl_b32 s77, s77, 11\n"
  "s_add_u32 s77, s77, s92\n"
  "buffer_load_dwordx4 v[144:147], v2, s[12:15], s77 offen offset:0 sc1\n"
  "buffer_load_dwordx4 v[148:151], v2, s[12:15], s77 offen offset:1024 sc1\n"
  "buffer_load_dwordx4 v[152:155], v206, s[12:15], s77 offen offset:0 sc1\n"
  "buffer_load_dwordx4 v[156:159], v206, s[12:15], s77 offen offset:1024 sc1\n"
  ".Lb2:\n"
  "s_add_u32 s77, s57, 2\n"
  "s_and_b32 s77, s77, 3\n"
  "s_lshl_b32 s77, s77, 11\n"
  "s_add_u32 s77, s77, s92\n"
  "buffer_load_dwordx4 v[160:163], v2, s[12:15], s77 offen offset:0 sc1\n"
  "buffer_load_dwordx4 v[164:167], v2, s[12:15], s77 offen offset:1024 sc1\n"
  "buffer_load_dwordx4 v[168:171], v206, s[12:15], s77 offen offset:0 sc1\n"
  "buffer_load_dwordx4 v[172:175], v206, s[12:15], s77 offen offset:1024 sc1\n"
  ".Lb3:\n"
  "s_add_u32 s77, s57, 3\n"
  "s_and_b32 s77, s77, 3\n"
  "s_lshl_b32 s77, s77, 11\n"
  "s_add_u32 s77, s77, s92\n"
  "buffer_load_dwordx4 v[176:179], v2, s[12:15], s77 offen offset:0 sc1\n"
  "buffer_load_dwordx4 v[180:183], v2, s[12:15], s77 offen offset:1024 sc1\n"
  "buffer_load_dwordx4 v[184:187], v206, s[12:15], s77 offen offset:0 sc1\n"
  "buffer_load_dwordx4 v[188:191], v206, s[12:15], s77 offen offset:1024 sc1\n"
  "s_branch .Lret\n"
  ".Lc0:\n"
  "s_add_u32 s77, s55, 0\n"
  "s_sub_u32 s64, s77, 7\n"
  "s_min_u32 s77, s77, s64\n"
  "s_lshl_b32 s77, s77, 12\n"
  "s_add_u32 s77, s77, s92\n"
  "buffer_load_dwordx4 v[128:131], v2, s[12:15], s77 offen offset:0 sc1\n"
  "buffer_load_dwordx4 v[132:135], v2, s[12:15], s77 offen offset:1024 sc1\n"
  "buffer_load_dwordx4 v[136:139], v2, s[12:15], s77 offen offset:2048 sc1\n"
  "buffer_load_dwordx4 v[140:143], v2, s[12:15], s77 offen offset:3072 sc1\n"
  ".Lc1:\n"
  "s_add_u32 s77, s55, 1\n"
  "s_sub_u32 s64, s77, 7\n"
  "s_min_u32 s77, s77, s64\n"
  "s_lshl_b32 s77, s77, 12\n"
  "s_add_u32 s77, s77, s92\n"
  "buffer_load_dwordx4 v[144:147], v2, s[12:15], s77 offen offset:0 sc1\n"
  "buffer_load_dwordx4 v[148:151], v2, s[12:15], s77 offen offset:1024 sc1\n"
  "buffer_load_dwordx4 v[152:155], v2, s[12:15], s77 offen offset:2048 sc1\n"
  "buffer_load_dwordx4 v[156:159], v2, s[12:15], s77 offen offset:3072 sc1\n"
  ".Lc2:\n"
  "s_add_u32 s77, s55, 2\n"
  "s_sub_u32 s64, s77, 7\n"
  "s_min_u32 s77, s77, s64\n"
  "s_lshl_b32 s77, s77, 12\n"
  "s_add_u32 s77, s77, s92\n"
  "buffer_load_dwordx4 v[160:163], v2, s[12:15], s77 offen offset:0 sc1\n"
  "buffer_load_dwordx4 v[164:167], v2, s[12:15], s77 offen offset:1024 sc1\n"
  "buffer_load_dwordx4 v[168:171], v2, s[12:15], s77 offen offset:2048 sc1\n"
  "buffer_load_dwordx4 v[172:175], v2, s[12:15], s77 offen offset:3072 sc1\n"
  ".Lc3:\n"
  "s_add_u32 s77, s55, 3\n"
  "s_sub_u32 s64, s77, 7\n"
  "s_min_u32 s77, s77, s64\n"
  "s_lshl_b32 s77, s77, 12\n"
  "s_add_u32 s77, s77, s92\n"
  "buffer_load_dwordx4 v[176:179], v2, s[12:15], s77 offen offset:0 sc1\n"
  "buffer_load_dwordx4 v[180:183], v2, s[12:15], s77 offen offset:1024 sc1\n"
  "buffer_load_dwordx4 v[184:187], v2, s[12:15], s77 offen offset:2048 sc1\n"
  "buffer_load_dwordx4 v[188:191], v2, s[12:15], s77 offen offset:3072 sc1\n"
  ".Lc4:\n"
  "s_add_u32 s77, s55, 4\n"
  "s_sub_u32 s64, s77, 7\n"
  "s_min_u32 s77, s77, s64\n"
  "s_lshl_b32 s77, s77, 12\n"
  "s_add_u32 s77, s77, s92\n"
  "buffer_load_dwordx4 v[192:195], v2, s[12:15], s77 offen offset:0 sc1\n"
  "buffer_load_dwordx4 v[196:199], v2, s[12:15], s77 offen offset:1024 sc1\n"
  "buffer_load_dwordx4 v[200:203], v2, s[12:15], s77 offen offset:2048 sc1\n"
  "buffer_load_dwordx4 v[204:207], v2, s[12:15], s77 offen offset:3072 sc1\n"
  ".Lc5:\n"
  "s_add_u32 s77, s55, 5\n"
  "s_sub_u32 s64, s77, 7\n"
  "s_min_u32 s77, s77, s64\n"
  "s_lshl_b32 s77, s77, 12\n"
  "s_add_u32 s77, s77, s92\n"
  "buffer_load_dwordx4 v[208:211], v2, s[12:15], s77 offen offset:0 sc1\n"
  "buffer_load_dwordx4 v[212:215], v2, s[12:15], s77 offen offset:1024 sc1\n"
  "buffer_load_dwordx4 v[216:219], v2, s[12:15], s77 offen offset:2048 sc1\n"
  "buffer_load_dwordx4 v[220:223], v2, s[12:15], s77 offen offset:3072 sc1\n"
  ".Lc7:\n"
  "buffer_load_dwordx4 v[48:51], v14, s[12:15], s91 offen offset:0 sc1\n"
  "buffer_load_dwordx4 v[52:55], v14, s[12:15], s91 offen offset:512 sc1\n"
  "buffer_load_dwordx4 v[56:59], v14, s[12:15], s91 offen offset:1024 sc1\n"
  "buffer_load_dwordx4 v[60:63], v14, s[12:15], s91 offen offset:1536 sc1\n"
  "buffer_load_dwordx4 v[240:243], v14, s[12:15], s91 offen offset:2048 sc1\n"
  "buffer_load_dwordx4 v[244:247], v14, s[12:15], s91 offen offset:2560 sc1\n"
  "buffer_load_dwordx4 v[248:251], v14, s[12:15], s91 offen offset:3072 sc1\n"
  ".Lc6:\n"
  "s_add_u32 s77, s55, 6\n"
  "s_sub_u32 s64, s77, 7\n"
  "s_min_u32 s77, s77, s64\n"
  "s_lshl_b32 s77, s77, 12\n"
  "s_add_u32 s77, s77, s92\n"
  "buffer_load_dwordx4 v[224:227], v2, s[12:15], s77 offen offset:0 sc1\n"
  "buffer_load_dwordx4 v[228:231], v2, s[12:15], s77 offen offset:1024 sc1\n"
  "buffer_load_dwordx4 v[232:235], v2, s[12:15], s77 offen offset:2048 sc1\n"
  "buffer_load_dwordx4 v[236:239], v2, s[12:15], s77 offen offset:3072 sc1\n"
  "s_branch .Lret\n"
  ".Ld0_0:\n"
  "s_add_u32 s77, s57, 0\n"
  "s_and_b32 s77, s77, 3\n"
  "s_mul_i32 s77, s77, 7168\n"
  "s_add_u32 s77, s77, s92\n"
  "buffer_load_dwordx4 v[240:243], v2, s[12:15], s77 offen offset:0 sc1\n"
  "buffer_load_dwordx4 v[244:247], v2, s[12:15], s77 offen offset:1024 sc1\n"
  "buffer_load_dwordx4 v[248:251], v11, s[12:15], s77 offen offset:0 sc1\n"
  "buffer_load_dwordx4 v[112:115], v11, s[12:15], s77 offen offset:1024 sc1\n"
  ".Ld0_1:\n"
  "s_add_u32 s77, s57, 1\n"
  "s_and_b32 s77, s77, 3\n"
  "s_mul_i32 s77, s77, 7168\n"
  "s_add_u32 s77, s77, s92\n"
  "buffer_load_dwordx4 v[116:119], v2, s[12:15], s77 offen offset:0 sc1\n"
  "buffer_load_dwordx4 v[120:123], v2, s[12:15], s77 offen offset:1024 sc1\n"
  "buffer_load_dwordx4 v[128:131], v11, s[12:15], s77 offen offset:0 sc1\n"
  "buffer_load_dwordx4 v[132:135], v11, s[12:15], s77 offen offset:1024 sc1\n"
  ".Ld0_2:\n"
  "s_add_u32 s77, s57, 2\n"
  "s_and_b32 s77, s77, 3\n"
  "s_mul_i32 s77, s77, 7168\n"
  "s_add_u32 s77, s77, s92\n"
  "buffer_load_dwordx4 v[136:139], v2, s[12:15], s77 offen offset:0 sc1\n"
  "buffer_load_dwordx4 v[140:143], v2, s[12:15], s77 offen offset:1024 sc1\n"
  "buffer_load_dwordx4 v[144:147], v11, s[12:15], s77 offen offset:0 sc1\n"
  "buffer_load_dwordx4 v[148:151], v11, s[12:15], s77 offen offset:1024 sc1\n"
  ".Ld0_3:\n"
  "s_add_u32 s77, s57, 3\n"
  "s_and_b32 s77, s77, 3\n"
  "s_mul_i32 s77, s77, 7168\n"
  "s_add_u32 s77, s77, s92\n"
  "buffer_load_dwordx4 v[152:155], v2, s[12:15], s77 offen offset:0 sc1\n"
  "buffer_load_dwordx4 v[156:159], v2, s[12:15], s77 offen offset:1024 sc1\n"
  "buffer_load_dwordx4 v[160:163], v11, s[12:15], s77 offen offset:0 sc1\n"
  "buffer_load_dwordx4 v[164:167], v11, s[12:15], s77 offen offset:1024 sc1\n"
  "s_branch .Lret\n"
  ".Ld1_0:\n"
  "s_add_u32 s77, s57, 0\n"
  "s_and_b32 s77, s77, 3\n"
  "s_mul_i32 s77, s77, 7168\n"
  "s_add_u32 s77, s77, s92\n"
  "buffer_load_dwordx4 v[168:171], v2, s[12:15], s77 offen offset:2048 sc1\n"
  "buffer_load_dwordx4 v[172:175], v2, s[12:15], s77 offen offset:3072 sc1\n"
  "buffer_load_dwordx4 v[176:179], v11, s[12:15], s77 offen offset:2048 sc1\n"
  "buffer_load_dwordx4 v[180:183], v11, s[12:15], s77 offen offset:3072 sc1\n"
  ".Ld1_1:\n"
  "s_add_u32 s77, s57, 1\n"
  "s_and_b32 s77, s77, 3\n"
  "s_mul_i32 s77, s77, 7168\n"
  "s_add_u32 s77, s77, s92\n"
  "buffer_load_dwordx4 v[184:187], v2, s[12:15], s77 offen offset:2048 sc1\n"
  "buffer_load_dwordx4 v[188:191], v2, s[12:15], s77 offen offset:3072 sc1\n"
  "buffer_load_dwordx4 v[192:195], v11, s[12:15], s77 offen offset:2048 sc1\n"
  "buffer_load_dwordx4 v[196:199], v11, s[12:15], s77 offen offset:3072 sc1\n"
  ".Ld1_2:\n"
  "s_add_u32 s77, s57, 2\n"
  "s_and_b32 s77, s77, 3\n"
  "s_mul_i32 s77, s77, 7168\n"
  "s_add_u32 s77, s77, s92\n"
  "buffer_load_dwordx4 v[200:203], v2, s[12:15], s77 offen offset:2048 sc1\n"
  "buffer_load_dwordx4 v[204:207], v2, s[12:15], s77 offen offset:3072 sc1\n"
  "buffer_load_dwordx4 v[208:211], v11, s[12:15], s77 offen offset:2048 sc1\n"
  "buffer_load_dwordx4 v[212:215], v11, s[12:15], s77 offen offset:3072 sc1\n"
  ".Ld1_3:\n"
  "s_add_u32 s77, s57, 3\n"
  "s_and_b32 s77, s77, 3\n"
  "s_mul_i32 s77, s77, 7168\n"
  "s_add_u32 s77, s77, s92\n"
  "buffer_load_dwordx4 v[216:219], v2, s[12:15], s77 offen offset:2048 sc1\n"
  "buffer_load_dwordx4 v[220:223], v2, s[12:15], s77 offen offset:3072 sc1\n"
  "buffer_load_dwordx4 v[224:227], v11, s[12:15], s77 offen offset:2048 sc1\n"
  "buffer_load_dwordx4 v[228:231], v11, s[12:15], s77 offen offset:3072 sc1\n"
  "s_branch .Lret\n"
  ".Ld2_0:\n"
  "s_add_u32 s77, s57, 0\n"
  "s_and_b32 s77, s77, 3\n"
  "s_mul_i32 s77, s77, 7168\n"
  "s_add_u32 s77, s77, s92\n"
  "s_add_u32 s77, s77, 4096\n"
  "buffer_load_dwordx4 v[232:235], v2, s[12:15], s77 offen offset:0 sc1\n"
  "buffer_load_dwordx4 v[236:239], v2, s[12:15], s77 offen offset:1024 sc1\n"
  "buffer_load_dwordx4 v[64:67], v11, s[12:15], s77 offen offset:0 sc1\n"
  "buffer_load_dwordx4 v[68:71], v11, s[12:15], s77 offen offset:1024 sc1\n"
  ".Ld2_1:\n"
  "s_add_u32 s77, s57, 1\n"
  "s_and_b32 s77, s77, 3\n"
  "s_mul_i32 s77, s77, 7168\n"
  "s_add_u32 s77, s77, s92\n"
  "s_add_u32 s77, s77, 4096\n"
  "buffer_load_dwordx4 v[72:75], v2, s[12:15], s77 offen offset:0 sc1\n"
  "buffer_load_dwordx4 v[76:79], v2, s[12:15], s77 offen offset:1024 sc1\n"
  "buffer_load_dwordx4 v[80:83], v11, s[12:15], s77 offen offset:0 sc1\n"
  "buffer_load_dwordx4 v[84:87], v11, s[12:15], s77 offen offset:1024 sc1\n"
  ".Ld2_2:\n"
  "s_add_u32 s77, s57, 2\n"
  "s_and_b32 s77, s77, 3\n"
  "s_mul_i32 s77, s77, 7168\n"
  "s_add_u32 s77, s77, s92\n"
  "s_add_u32 s77, s77, 4096\n"
  "buffer_load_dwordx4 v[88:91], v2, s[12:15], s77 offen offset:0 sc1\n"
  "buffer_load_dwordx4 v[92:95], v2, s[12:15], s77 offen offset:1024 sc1\n"
  "buffer_load_dwordx4 v[48:51], v11, s[12:15], s77 offen offset:0 sc1\n"
  "buffer_load_dwordx4 v[52:55], v11, s[12:15], s77 offen offset:1024 sc1\n"
  ".Ld2_3:\n"
  "s_add_u32 s77, s57, 3\n"
  "s_and_b32 s77, s77, 3\n"
  "s_mul_i32 s77, s77, 7168\n"
  "s_add_u32 s77, s77, s92\n"
  "s_add_u32 s77, s77, 4096\n"
  "buffer_load_dwordx4 v[56:59], v2, s[12:15], s77 offen offset:0 sc1\n"
  "buffer_load_dwordx4 v[60:63], v2, s[12:15], s77 offen offset:1024 sc1\n"
  "buffer_load_dwordx4 v[240:243], v11, s[12:15], s77 offen offset:0 sc1\n"
  "buffer_load_dwordx4 v[244:247], v11, s[12:15], s77 offen offset:1024 sc1\n"
  "s_branch .Lret\n"
  ".Ld3_0:\n"
  "s_add_u32 s77, s57, 0\n"
  "s_and_b32 s77, s77, 3\n"
  "s_mul_i32 s77, s77, 7168\n"
  "s_add_u32 s77, s77, s92\n"
  "s_add_u32 s77, s77, 4096\n"
  "buffer_load_dwordx4 v[248:251], v2, s[12:15], s77 offen offset:2048 sc1\n"
  "s_add_u32 s77, s57, 1\n"
  "s_and_b32 s77, s77, 3\n"
  "s_mul_i32 s77, s77, 7168\n"
  "s_add_u32 s77, s77, s92\n"
  "s_add_u32 s77, s77, 4096\n"
  "buffer_load_dwordx4 v[112:115], v2, s[12:15], s77 offen offset:2048 sc1\n"
  "s_add_u32 s77, s57, 2\n"
  "s_and_b32 s77, s77, 3\n"
  "s_mul_i32 s77, s77, 7168\n"
  "s_add_u32 s77, s77, s92\n"
  "s_add_u32 s77, s77, 4096\n"
  "buffer_load_dwordx4 v[116:119], v2, s[12:15], s77 offen offset:2048 sc1\n"
  "s_add_u32 s77, s57, 3\n"
  "s_and_b32 s77, s77, 3\n"
  "s_mul_i32 s77, s77, 7168\n"
  "s_add_u32 s77, s77, s92\n"
  "s_add_u32 s77, s77, 4096\n"
  "buffer_load_dwordx4 v[120:123], v2, s[12:15], s77 offen offset:2048 sc1\n"
  ".Ld3_1:\n"
  "s_add_u32 s77, s57, 0\n"
  "s_and_b32 s77, s77, 3\n"
  "s_mul_i32 s77, s77, 7168\n"
  "s_add_u32 s77, s77, s92\n"
  "s_add_u32 s77, s77, 4096\n"
  "buffer_load_dwordx4 v[128:131], v11, s[12:15], s77 offen offset:2048 sc1\n"
  "s_add_u32 s77, s57, 1\n"
  "s_and_b32 s77, s77, 3\n"
  "s_mul_i32 s77, s77, 7168\n"
  "s_add_u32 s77, s77, s92\n"
  "s_add_u32 s77, s77, 4096\n"
  "buffer_load_dwordx4 v[132:135], v11, s[12:15], s77 offen offset:2048 sc1\n"
  "s_add_u32 s77, s57, 2\n"
  "s_and_b32 s77, s77, 3\n"
  "s_mul_i32 s77, s77, 7168\n"
  "s_add_u32 s77, s77, s92\n"
  "s_add_u32 s77, s77, 4096\n"
  "buffer_load_dwordx4 v[136:139], v11, s[12:15], s77 offen offset:2048 sc1\n"
  "s_add_u32 s77, s57, 3\n"
  "s_and_b32 s77, s77, 3\n"
  "s_mul_i32 s77, s77, 7168\n"
  "s_add_u32 s77, s77, s92\n"
  "s_add_u32 s77, s77, 4096\n"
  "buffer_load_dwordx4 v[140:143], v11, s[12:15], s77 offen offset:2048 sc1\n"
  "s_branch .Lret\n"
  ".Lret:\n"
  "s_waitcnt vmcnt(0)\n"
  "s_sub_u32 s76, s76, 1\n"
  "s_cselect_b64 s[48:49], 0, s[48:49]\n"
  "s_setpc_b64 s[46:47]\n"
  ".Lk_end:\n"
  :: "s"(__builtin_amdgcn_kernarg_segment_ptr()), "s"(blockIdx.x), "v"(threadIdx.x)
  : "memory", "vcc", "scc", "s3", "s4", "s5", "s6", "s7", "s8", "s9", "s10", "s11", "s12", "s13", "s14", "s15", "s16", "s17", "s18", "s19", "s20", "s21", "s22", "s23", "s24", "s25", "s26", "s27", "s28", "s29", "s30", "s31", "s32", "s33", "s34", "s35", "s36", "s37", "s38", "s39", "s40", "s41", "s42", "s43", "s44", "s45", "s46", "s47", "s48", "s49", "s50", "s51", "s52", "s53", "s54", "s55", "s56", "s57", "s58", "s59", "s60", "s61", "s62", "s63", "s64", "s65", "s66", "s67", "s68", "s69", "s70", "s71", "s72", "s73", "s74", "s75", "s76", "s77", "s78", "s79", "s80", "s81", "s82", "s83", "s84", "s85", "s86", "s87", "s88", "s89", "s90", "s91", "s92", "s93", "s94", "s95", "s96", "s97", "s98", "s99", "s100", "s101", "v1", "v2", "v3", "v4", "v5", "v6", "v7", "v8", "v9", "v10", "v11", "v12", "v13", "v14", "v15", "v16", "v17", "v18", "v19", "v20", "v21", "v22", "v23", "v24", "v25", "v26", "v27", "v28", "v29", "v30", "v31", "v32", "v33", "v34", "v35", "v36", "v37", "v38", "v39", "v40", "v41", "v42", "v43", "v44", "v45", "v46", "v47", "v48", "v49", "v50", "v51", "v52", "v53", "v54", "v55", "v56", "v57", "v58", "v59", "v60", "v61", "v62", "v63", "v64", "v65", "v66", "v67", "v68", "v69", "v70", "v71", "v72", "v73", "v74", "v75", "v76", "v77", "v78", "v79", "v80", "v81", "v82", "v83", "v84", "v85", "v86", "v87", "v88", "v89", "v90", "v91", "v92", "v93", "v94", "v95", "v96", "v97", "v98", "v99", "v100", "v101", "v102", "v103", "v104", "v105", "v106", "v107", "v108", "v109", "v110", "v111", "v112", "v113", "v114", "v115", "v116", "v117", "v118", "v119", "v120", "v121", "v122", "v123", "v124", "v125", "v126", "v127", "v128", "v129", "v130", "v131", "v132", "v133", "v134", "v135", "v136", "v137", "v138", "v139", "v140", "v141", "v142", "v143", "v144", "v145", "v146", "v147", "v148", "v149", "v150", "v151", "v152", "v153", "v154", "v155", "v156", "v157", "v158", "v159", "v160", "v161", "v162", "v163", "v164", "v165", "v166", "v167", "v168", "v169", "v170", "v171", "v172", "v173", "v174", "v175", "v176", "v177", "v178", "v179", "v180", "v181", "v182", "v183", "v184", "v185", "v186", "v187", "v188", "v189", "v190", "v191", "v192", "v193", "v194", "v195", "v196", "v197", "v198", "v199", "v200", "v201", "v202", "v203", "v204", "v205", "v206", "v207", "v208", "v209", "v210", "v211", "v212", "v213", "v214", "v215", "v216", "v217", "v218", "v219", "v220", "v221", "v222", "v223", "v224", "v225", "v226", "v227", "v228", "v229", "v230", "v231", "v232", "v233", "v234", "v235", "v236", "v237", "v238", "v239", "v240", "v241", "v242", "v243", "v244", "v245", "v246", "v247", "v248", "v249", "v250", "v251", "v252", "v253", "v254", "v255", "a0", "a1", "a2", "a3", "a4", "a5", "a6", "a7", "a8", "a9", "a10", "a11", "a12", "a13", "a14", "a15", "a16", "a17", "a18", "a19", "a20", "a21", "a22", "a23", "a24", "a25", "a26", "a27", "a28", "a29", "a30", "a31", "a32", "a33", "a34", "a35", "a36", "a37", "a38", "a39", "a40", "a41", "a42", "a43", "a44", "a45", "a46", "a47", "a48", "a49", "a50", "a51", "a52", "a53", "a54", "a55", "a56", "a57", "a58", "a59", "a60", "a61", "a62", "a63", "a64", "a65", "a66", "a67", "a68", "a69", "a70", "a71", "a72", "a73", "a74", "a75", "a76", "a77", "a78", "a79", "a80", "a81", "a82", "a83", "a84", "a85", "a86", "a87", "a88", "a89", "a90", "a91", "a92", "a93", "a94", "a95", "a96", "a97", "a98", "a99", "a100", "a101", "a102", "a103", "a104", "a105", "a106", "a107", "a108", "a109", "a110", "a111", "a112", "a113", "a114", "a115", "a116", "a117", "a118", "a119", "a120", "a121", "a122", "a123", "a124", "a125", "a126", "a127", "a128", "a129", "a130", "a131", "a132", "a133", "a134", "a135", "a136", "a137", "a138", "a139", "a140", "a141", "a142", "a143", "a144", "a145", "a146", "a147", "a148", "a149", "a150", "a151", "a152", "a153", "a154", "a155", "a156", "a157", "a158", "a159", "a160", "a161", "a162", "a163", "a164", "a165", "a166", "a167", "a168", "a169", "a170", "a171", "a172", "a173", "a174", "a175", "a176", "a177", "a178", "a179", "a180", "a181", "a182", "a183", "a184", "a185", "a186", "a187", "a188", "a189", "a190", "a191", "a192", "a193", "a194", "a195", "a196", "a197", "a198", "a199", "a200", "a201", "a202", "a203", "a204", "a205", "a206", "a207", "a208", "a209", "a210", "a211", "a212", "a213", "a214", "a215", "a216", "a217", "a218", "a219", "a220", "a221", "a222", "a223", "a224", "a225", "a226", "a227", "a228", "a229", "a230", "a231", "a232", "a233", "a234", "a235", "a236", "a237", "a238", "a239", "a240", "a241", "a242", "a243", "a244", "a245", "a246", "a247", "a248", "a249", "a250", "a251", "a252", "a253", "a254", "a255");
}

extern "C" void kernel_launch(void* const* d_in, const int* in_sizes, int n_in, void* d_out, int out_size, void* d_ws, size_t ws_size, hipStream_t stream) {
  const unsigned F = 0x20000u;
  (void)hipFuncSetAttribute((const void*)fused_block, hipFuncAttributeMaxDynamicSharedMemorySize, 163840);
  fused_block<<<256, 256, 163840, stream>>>(d_in[0], 2048u * 896u, F, d_in[1], 448u * 2688u, F, d_ws, WS_END, F, d_in[7], 1792u * 896u, F, d_in[3], 448u * 896u, F,
                                            d_in[2], 16u * 1568u, F, d_in[5], 448u * 3584u, F, d_in[6], 3584u, F, d_in[8], 896u, F, d_out, 2048u * 896u, F);
}
```
